```python
import torch

def _softmax(x, axis=-1):
    return torch.softmax(x, dim=axis)
def _gelu(x):
    return torch.nn.functional.gelu(x, approximate="tanh")
def _silu(x):
    return torch.nn.functional.silu(x)
def _sigmoid(x):
    return torch.sigmoid(x)
def _layernorm(x, axis=-1, eps=1e-5):
    m = x.mean(dim=axis, keepdim=True)
    v = x.var(dim=axis, keepdim=True, unbiased=False)
    return (x - m) / torch.sqrt(v + eps)
def _scatter_add(tgt, idx, src, axis=0):
    return tgt.clone().index_add_(axis, idx, src)
def _segment_sum(src, seg, n):
    out = torch.zeros((n,) + src.shape[1:], dtype=src.dtype)
    return out.index_add_(0, seg, src)
def _pad(x, pw):
    flat = tuple(v for pair in reversed(pw) for v in pair)
    return torch.nn.functional.pad(x, flat)

kernel_name = 'standalone__kv_cache_quant_int4_int2_kv_with_per_channel_or_per_token_scales__decode'

def setup_inputs():

    _w = (lambda a: (lambda t: t.to(torch.bfloat16) if t.is_floating_point() else t)(torch.as_tensor(a)))
    B = 4
    Sq = 1
    Skv = 8192
    Hq = 32
    Hkv = 8
    Dh = 128
    bits = 2
    G = 64
    if G <= 0 or G >= Skv:
        G = max(1, Skv // 2)
    Skv = (Skv // G) * G
    ngk = Skv // G
    qmax = (1 << bits) - 1
    return {
        "q": _w((lambda *s: torch.randn(*s, dtype=torch.bfloat16) * 3.2)(B, Hq, Sq, Dh)),
        "qk": _w(torch.randint(0, qmax + 1, (B, Hkv, Skv, Dh)).to(torch.int32)),
        "qv": _w(torch.randint(0, qmax + 1, (B, Hkv, Skv, Dh)).to(torch.int32)),
        "k_scales": _w(torch.abs((lambda *s: torch.randn(*s, dtype=torch.bfloat16) * 3.2)(B, Hkv, ngk, Dh)) * 0.05 + 0.02),
        "k_zeros": _w((lambda *s: torch.randn(*s, dtype=torch.bfloat16) * 3.2)(B, Hkv, ngk, Dh) + float(qmax) / 2.0),
        "v_scales": _w(torch.abs((lambda *s: torch.randn(*s, dtype=torch.bfloat16) * 0.05)(B, Hkv, Skv, 1)) * 0.05 + 0.02),
        "v_zeros": _w((lambda *s: torch.randn(*s, dtype=torch.bfloat16) * 0.05)(B, Hkv, Skv, 1) + float(qmax) / 2.0),
    }

def reference(q, qk, qv, k_scales, k_zeros, v_scales, v_zeros):
    B, Hq, Sq, Dh = q.shape
    Hkv, Skv = qk.shape[1], qk.shape[2]
    ngk = k_scales.shape[2]
    G = Skv // ngk
    ks = (lambda x, n, axis: torch.repeat_interleave(x, n, dim=axis))(k_scales, G, axis=2)
    kz = (lambda x, n, axis: torch.repeat_interleave(x, n, dim=axis))(k_zeros, G, axis=2)
    k = ((lambda x, dt: x.to(dt))(qk, q.dtype) - kz) * ks
    v = ((lambda x, dt: x.to(dt))(qv, q.dtype) - v_zeros) * v_scales
    rep = Hq // Hkv
    if rep > 1:
        k = (lambda x, n, axis: torch.repeat_interleave(x, n, dim=axis))(k, rep, axis=1)
        v = (lambda x, n, axis: torch.repeat_interleave(x, n, dim=axis))(v, rep, axis=1)
    scale = float(Dh) ** -0.5
    logits = torch.einsum("bhqd,bhkd->bhqk", q, k) * scale
    p = _softmax(logits, axis=-1)
    o = torch.einsum("bhqk,bhkd->bhqd", p, v)
    return o
```

```cpp
#include <hip/hip_runtime.h>
#include <hip/hip_bf16.h>
#include <stdint.h>

#define WAVES 8
#define KV_NT 1

constexpr int B_ = 4, HQ = 32, HKV = 8, SKV = 8192, D = 128, GRP = 64, NG = SKV / GRP, REP = HQ / HKV;
constexpr int NBH = B_ * HKV;
constexpr int NSPLIT = NG / WAVES;
constexpr int NTHR = WAVES * 64;

typedef int i32x4 __attribute__((ext_vector_type(4)));
typedef unsigned u32x4 __attribute__((ext_vector_type(4)));
typedef unsigned u32x2 __attribute__((ext_vector_type(2)));
typedef float f32x4 __attribute__((ext_vector_type(4)));
typedef float f32x2 __attribute__((ext_vector_type(2)));
typedef __bf16 bf16x8 __attribute__((ext_vector_type(8)));
typedef __bf16 bf16x2 __attribute__((ext_vector_type(2)));

__device__ __forceinline__ i32x4 ldkv(const int* p) {
  return __builtin_nontemporal_load(reinterpret_cast<const i32x4*>(p));
}
__device__ __forceinline__ unsigned pk_bf16(float lo, float hi) {
  f32x2 v = {lo, hi};
  bf16x2 r = __builtin_convertvector(v, bf16x2);
  return __builtin_bit_cast(unsigned, r);
}
__device__ __forceinline__ float bf_lo(unsigned u) { return __builtin_bit_cast(float, u << 16); }
__device__ __forceinline__ float bf_hi(unsigned u) { return __builtin_bit_cast(float, u & 0xffff0000u); }
__device__ __forceinline__ f32x4 mfma(u32x4 a, u32x4 b, f32x4 c) {
  return __builtin_amdgcn_mfma_f32_16x16x32_bf16(__builtin_bit_cast(bf16x8, a), __builtin_bit_cast(bf16x8, b), c, 0, 0, 0);
}
template <int CTRL> __device__ __forceinline__ float dpp(float x) {
  return __builtin_bit_cast(float, __builtin_amdgcn_mov_dpp(__builtin_bit_cast(int, x), CTRL, 0xf, 0xf, true));
}
constexpr int ROR4 = 0x124;
__device__ __forceinline__ float xg_max(float x) {
  auto s = __builtin_amdgcn_permlane16_swap(__float_as_uint(x), __float_as_uint(x), false, false);
  x = fmaxf(__uint_as_float(s[0]), __uint_as_float(s[1]));
  auto t = __builtin_amdgcn_permlane32_swap(__float_as_uint(x), __float_as_uint(x), false, false);
  return fmaxf(__uint_as_float(t[0]), __uint_as_float(t[1]));
}
__device__ __forceinline__ float xg_sum(float x) {
  auto s = __builtin_amdgcn_permlane16_swap(__float_as_uint(x), __float_as_uint(x), false, false);
  x = __uint_as_float(s[0]) + __uint_as_float(s[1]);
  auto t = __builtin_amdgcn_permlane32_swap(__float_as_uint(x), __float_as_uint(x), false, false);
  return __uint_as_float(t[0]) + __uint_as_float(t[1]);
}

#define SB() __builtin_amdgcn_sched_barrier(0)

__global__ void __launch_bounds__(NTHR, 2)
kv_split(const uint16_t* __restrict__ q, const int* __restrict__ qk, const int* __restrict__ qv,
         const uint16_t* __restrict__ ks, const uint16_t* __restrict__ kz,
         const uint16_t* __restrict__ vs, const uint16_t* __restrict__ vz,
         float* __restrict__ po, float* __restrict__ pml) {
  __shared__ __attribute__((aligned(16))) float lds_o[WAVES][2][4][D];
  __shared__ float lds_m[WAVES][4], lds_l[WAVES][4];
  const int bh = blockIdx.x / NSPLIT, split = blockIdx.x % NSPLIT;
  const int tid = threadIdx.x, lane = tid & 63;
  const int wave = __builtin_amdgcn_readfirstlane(tid >> 6);
  const int c = lane & 15, g4 = lane >> 4;
  const int grp = split * WAVES + wave;
  const size_t tok0 = (size_t)bh * SKV + (size_t)grp * GRP;
  const int* kp = qk + tok0 * D + (16 * (c >> 2) + (c & 3)) * D + 4 * g4;
  const int* vp = qv + tok0 * D + (16 * g4) * D + 4 * c;

  i32x4 buf[2][8];
#pragma unroll
  for (int n = 0; n < 8; ++n) buf[0][n] = ldkv(kp + 0 * 4 * D + n * 16);
#pragma unroll
  for (int n = 0; n < 8; ++n) buf[1][n] = ldkv(kp + 1 * 4 * D + n * 16);
  SB();
  const uint16_t* qp = q + ((size_t)bh * REP + (c & 3)) * D + 4 * g4;
  const size_t sgo = ((size_t)bh * NG + grp) * D + 4 * g4;
  u32x2 qr[8], sr[8], zr[8];
#pragma unroll
  for (int n = 0; n < 8; ++n) {
    qr[n] = *reinterpret_cast<const u32x2*>(qp + 16 * n);
    sr[n] = *reinterpret_cast<const u32x2*>(ks + sgo + 16 * n);
    zr[n] = *reinterpret_cast<const u32x2*>(kz + sgo + 16 * n);
  }
  u32x4 vsr[2], vzr[2];
#pragma unroll
  for (int pp = 0; pp < 2; ++pp) {
    vsr[pp] = *reinterpret_cast<const u32x4*>(vs + tok0 + 16 * g4 + 8 * pp);
    vzr[pp] = *reinterpret_cast<const u32x4*>(vz + tok0 + 16 * g4 + 8 * pp);
  }
  const bool lo_lane = (c >> 2) & 1;
  u32x4 Bq[4];
  f32x4 sinit = {0.f, 0.f, 0.f, 0.f};
#pragma unroll
  for (int m = 0; m < 4; ++m) {
    u32x4 Z;
#pragma unroll
    for (int u = 0; u < 2; ++u) {
      const int n = 2 * m + u;
#pragma unroll
      for (int w = 0; w < 2; ++w) {
        float f0 = bf_lo(qr[n][w]) * bf_lo(sr[n][w]);
        float f1 = bf_hi(qr[n][w]) * bf_hi(sr[n][w]);
        unsigned h = pk_bf16(f0, f1);
        unsigned l = pk_bf16(f0 - bf_lo(h), f1 - bf_hi(h));
        Bq[m][2 * u + w] = lo_lane ? l : h;
        Z[2 * u + w] = zr[n][w] ^ 0x80008000u;
      }
    }
    sinit = mfma(Z, Bq[m], sinit);
  }
  SB();
  f32x4 S[4];
#pragma unroll
  for (int tt = 0; tt < 4; ++tt) {
    f32x4 acc = sinit;
#pragma unroll
    for (int m = 0; m < 4; ++m) {
      const i32x4 a = buf[tt & 1][2 * m], b = buf[tt & 1][2 * m + 1];
      u32x4 A = {pk_bf16((float)a[0], (float)a[1]), pk_bf16((float)a[2], (float)a[3]),
                 pk_bf16((float)b[0], (float)b[1]), pk_bf16((float)b[2], (float)b[3])};
      SB();
      if (tt < 2) {
        buf[tt & 1][2 * m] = ldkv(kp + (tt + 2) * 4 * D + (2 * m) * 16);
        buf[tt & 1][2 * m + 1] = ldkv(kp + (tt + 2) * 4 * D + (2 * m + 1) * 16);
      } else {
        const int ch = tt - 2;
        buf[tt & 1][2 * m] = ldkv(vp + (4 * ch + m) * D);
        buf[tt & 1][2 * m + 1] = ldkv(vp + (4 * ch + m) * D + 64);
      }
      SB();
      acc = mfma(A, Bq[m], acc);
    }
    S[tt] = acc;
  }
  constexpr float C2 = 0.088388347648318440f * 1.4426950408889634f;
  float t[16];
#pragma unroll
  for (int tt = 0; tt < 4; ++tt)
#pragma unroll
    for (int r = 0; r < 4; ++r) t[4 * tt + r] = (S[tt][r] + dpp<ROR4>(S[tt][r])) * C2;
  float mx = t[0];
#pragma unroll
  for (int e = 1; e < 16; ++e) mx = fmaxf(mx, t[e]);
  mx = xg_max(mx);
  float ls = 0.f;
  u32x4 P[2];
#pragma unroll
  for (int e = 0; e < 16; e += 2) {
    const float p0 = __builtin_amdgcn_exp2f(t[e] - mx), p1 = __builtin_amdgcn_exp2f(t[e + 1] - mx);
    ls += p0 + p1;
    const unsigned sv = vsr[e >> 3][(e & 7) >> 1];
    const float f0 = p0 * bf_lo(sv), f1 = p1 * bf_hi(sv);
    const unsigned h = pk_bf16(f0, f1);
    const unsigned l = pk_bf16(f0 - bf_lo(h), f1 - bf_hi(h));
    P[e >> 3][(e & 7) >> 1] = lo_lane ? l : h;
  }
  ls = xg_sum(ls);
  f32x4 oinit = {0.f, 0.f, 0.f, 0.f};
#pragma unroll
  for (int pp = 0; pp < 2; ++pp) oinit = mfma(P[pp], vzr[pp] ^ 0x80008000u, oinit);
  f32x4 O[2][4];
#pragma unroll
  for (int hf = 0; hf < 2; ++hf)
#pragma unroll
    for (int cc = 0; cc < 4; ++cc) O[hf][cc] = oinit;
  SB();
#pragma unroll
  for (int pp = 0; pp < 2; ++pp) {
    u32x4 Bv[2][4];
#pragma unroll
    for (int ch = 0; ch < 2; ++ch) {
#pragma unroll
      for (int mm = 0; mm < 2; ++mm) {
#pragma unroll
        for (int hf = 0; hf < 2; ++hf) {
          const i32x4 a = buf[ch][2 * (2 * mm) + hf], b = buf[ch][2 * (2 * mm + 1) + hf];
#pragma unroll
          for (int cc = 0; cc < 4; ++cc) Bv[hf][cc][2 * ch + mm] = pk_bf16((float)a[cc], (float)b[cc]);
        }
        SB();
        if (pp == 0) {
#pragma unroll
          for (int u = 0; u < 2; ++u)
#pragma unroll
            for (int hf = 0; hf < 2; ++hf)
              buf[ch][2 * (2 * mm + u) + hf] = ldkv(vp + (8 + 4 * ch + 2 * mm + u) * D + 64 * hf);
        }
        SB();
      }
    }
#pragma unroll
    for (int hf = 0; hf < 2; ++hf)
#pragma unroll
      for (int cc = 0; cc < 4; ++cc) O[hf][cc] = mfma(P[pp], Bv[hf][cc], O[hf][cc]);
  }
  if (g4 < 2) {
#pragma unroll
    for (int hf = 0; hf < 2; ++hf)
#pragma unroll
      for (int r = 0; r < 4; ++r)
        *reinterpret_cast<f32x4*>(&lds_o[wave][g4][r][64 * hf + 4 * c]) = f32x4{O[hf][0][r], O[hf][1][r], O[hf][2][r], O[hf][3][r]};
  }
  if (lane < 4) { lds_m[wave][lane] = mx; lds_l[wave][lane] = ls; }
  __syncthreads();
  for (int idx = tid; idx < 4 * D; idx += NTHR) {
    const int h = idx >> 7, d = idx & 127;
    float M = lds_m[0][h];
#pragma unroll
    for (int w = 1; w < WAVES; ++w) M = fmaxf(M, lds_m[w][h]);
    float L = 0.f, o = 0.f;
#pragma unroll
    for (int w = 0; w < WAVES; ++w) {
      const float f = __builtin_amdgcn_exp2f(lds_m[w][h] - M);
      L = fmaf(f, lds_l[w][h], L);
      o = fmaf(f, lds_o[w][0][h][d] + lds_o[w][1][h][d], o);
    }
    const size_t slot = ((size_t)bh * REP + h) * NSPLIT + split;
    po[slot * D + d] = o;
    if (d == 0) { pml[2 * slot] = M; pml[2 * slot + 1] = L; }
  }
}

__global__ void __launch_bounds__(128)
kv_combine(const float* __restrict__ po, const float* __restrict__ pml, __hip_bfloat16* __restrict__ out) {
  const int row = blockIdx.x, d = threadIdx.x;
  float M = -INFINITY;
#pragma unroll
  for (int s = 0; s < NSPLIT; ++s) M = fmaxf(M, pml[2 * (row * NSPLIT + s)]);
  float L = 0.f, o = 0.f;
#pragma unroll
  for (int s = 0; s < NSPLIT; ++s) {
    const float f = __builtin_amdgcn_exp2f(pml[2 * (row * NSPLIT + s)] - M);
    L = fmaf(f, pml[2 * (row * NSPLIT + s) + 1], L);
    o = fmaf(f, po[(size_t)(row * NSPLIT + s) * D + d], o);
  }
  out[row * D + d] = __float2bfloat16(o / L);
}

extern "C" void kernel_launch(void* const* d_in, const int* in_sizes, int n_in, void* d_out, int out_size,
                              void* d_ws, size_t ws_size, hipStream_t stream) {
  const uint16_t* q = (const uint16_t*)d_in[0];
  const int* qk = (const int*)d_in[1];
  const int* qv = (const int*)d_in[2];
  const uint16_t* ks = (const uint16_t*)d_in[3];
  const uint16_t* kz = (const uint16_t*)d_in[4];
  const uint16_t* vs = (const uint16_t*)d_in[5];
  const uint16_t* vz = (const uint16_t*)d_in[6];
  float* pml = (float*)d_ws;
  float* po = (float*)((char*)d_ws + 65536);
  kv_split<<<NBH * NSPLIT, NTHR, 0, stream>>>(q, qk, qv, ks, kz, vs, vz, po, pml);
  kv_combine<<<B_ * HQ, 128, 0, stream>>>(po, pml, (__hip_bfloat16*)d_out);
}
```

```cpp
#include <hip/hip_runtime.h>
#include <hip/hip_bf16.h>
#include <stdint.h>

#define WAVES 4
#define WIN 8
#define ROT 1

constexpr int B_ = 4, HQ = 32, HKV = 8, SKV = 8192, D = 128, GRP = 64, NG = SKV / GRP, REP = HQ / HKV;
constexpr int NBH = B_ * HKV;
constexpr int NSPLIT = 8, NWG = NBH * NSPLIT;
constexpr int TOKWG = SKV / NSPLIT;
constexpr int NTHR = WAVES * 64;
constexpr int NIT = TOKWG / 16 / WAVES;
constexpr unsigned KVBYTES = (unsigned)B_ * HKV * SKV * D * 4u;

constexpr int L_KS = 0, L_KZ = 4096, L_VS = 8192, L_VZ = 10240, L_T = 12288;
constexpr int TROW = 272, TSZ = 16 * TROW;
constexpr int L_END = L_T + WAVES * TSZ;

typedef int i32x4 __attribute__((ext_vector_type(4)));
typedef unsigned u32x4 __attribute__((ext_vector_type(4)));
typedef unsigned u32x2 __attribute__((ext_vector_type(2)));
typedef float f32x4 __attribute__((ext_vector_type(4)));
typedef float f32x2 __attribute__((ext_vector_type(2)));
typedef float f32x16 __attribute__((ext_vector_type(16)));
typedef __bf16 bf16x8 __attribute__((ext_vector_type(8)));
typedef __bf16 bf16x2 __attribute__((ext_vector_type(2)));

__device__ __forceinline__ unsigned pk_bf16(float lo, float hi) {
  f32x2 v = {lo, hi};
  return __builtin_bit_cast(unsigned, __builtin_convertvector(v, bf16x2));
}
__device__ __forceinline__ float rnd_bf16(float x) {
  return __builtin_bit_cast(float, pk_bf16(0.f, x));
}
__device__ __forceinline__ float bf_lo(unsigned u) { return __builtin_bit_cast(float, u << 16); }
__device__ __forceinline__ float bf_hi(unsigned u) { return __builtin_bit_cast(float, u & 0xffff0000u); }
__device__ __forceinline__ f32x4 mfma16(u32x4 a, u32x4 b, f32x4 c) {
  return __builtin_amdgcn_mfma_f32_16x16x32_bf16(__builtin_bit_cast(bf16x8, a), __builtin_bit_cast(bf16x8, b), c, 0, 0, 0);
}
__device__ __forceinline__ f32x16 mfma32(u32x4 a, u32x4 b, f32x16 c) {
  return __builtin_amdgcn_mfma_f32_32x32x16_bf16(__builtin_bit_cast(bf16x8, a), __builtin_bit_cast(bf16x8, b), c, 0, 0, 0);
}
__device__ __forceinline__ float xg_max(float x) {
  auto s = __builtin_amdgcn_permlane16_swap(__float_as_uint(x), __float_as_uint(x), false, false);
  x = fmaxf(__uint_as_float(s[0]), __uint_as_float(s[1]));
  auto t = __builtin_amdgcn_permlane32_swap(__float_as_uint(x), __float_as_uint(x), false, false);
  return fmaxf(__uint_as_float(t[0]), __uint_as_float(t[1]));
}
__device__ __forceinline__ float xg_sum(float x) {
  auto s = __builtin_amdgcn_permlane16_swap(__float_as_uint(x), __float_as_uint(x), false, false);
  x = __uint_as_float(s[0]) + __uint_as_float(s[1]);
  auto t = __builtin_amdgcn_permlane32_swap(__float_as_uint(x), __float_as_uint(x), false, false);
  return __uint_as_float(t[0]) + __uint_as_float(t[1]);
}
#define SB() __builtin_amdgcn_sched_barrier(0)

__global__ void __launch_bounds__(NTHR, 2)
kv_split(const uint16_t* __restrict__ q, const int* __restrict__ qk, const int* __restrict__ qv,
         const uint16_t* __restrict__ ks, const uint16_t* __restrict__ kz,
         const uint16_t* __restrict__ vs, const uint16_t* __restrict__ vz,
         float* __restrict__ po, float* __restrict__ pml) {
  __shared__ __attribute__((aligned(16))) char lds[L_END];
  const int bid = (blockIdx.x & 7) * (NWG / 8) + (blockIdx.x >> 3);
  const int bh = bid / NSPLIT, split = bid % NSPLIT;
  const int tid = threadIdx.x, lane = tid & 63;
  const int wave = __builtin_amdgcn_readfirstlane(tid >> 6);
  const int c = lane & 15, g4 = lane >> 4, hh = lane >> 5, ch = lane & 31;
  const unsigned tokb = (unsigned)bh * SKV + (unsigned)split * TOKWG;
  const unsigned rot = ROT ? (blockIdx.x >> 3) : 0;

  u32x4 st[3];
  if (tid < 256) {
    const size_t go = ((size_t)bh * NG + split * (TOKWG / GRP)) * D;
    st[0] = **reinterpret_cast<const u32x4(*)[1]>(ks + go + tid * 8);
    st[1] = **reinterpret_cast<const u32x4(*)[1]>(kz + go + tid * 8);
    st[2] = **reinterpret_cast<const u32x4(*)[1]>((tid < 128 ? vs : vz) + tokb + (tid & 127) * 8);
  }
  u32x4 Bq[4];
#pragma unroll
  for (int m = 0; m < 4; ++m)
    Bq[m] = *reinterpret_cast<const u32x4*>(q + ((size_t)bh * REP + (c & 3)) * D + 32 * m + 8 * g4);
  SB();

  const unsigned vo0 = lane * 16, vo1 = lane * 16 + 4096;
  u32x4 buf[16];
  auto tile_of = [&](int it) -> unsigned { return (((unsigned)it + rot) & (NIT - 1)) * WAVES + wave; };
  auto issue = [&](int slot, const unsigned so, const bool live) __attribute__((always_inline)) {
    __amdgpu_buffer_rsrc_t r = __builtin_amdgcn_make_buffer_rsrc((void*)(slot < 8 ? qk : qv), 0, live ? (int)KVBYTES : 0, 0x00020000);
    const int n = slot & 7;
    buf[slot] = __builtin_amdgcn_raw_buffer_load_b128(r, (n < 4 ? vo0 : vo1) + (n & 3) * 1024, so, 2);
  };
  unsigned so_cur = (tokb + tile_of(0) * 16) * 512u;
#pragma unroll
  for (int i = 0; i < WIN; ++i) issue(i, so_cur, true);
  SB();
  if (tid < 256) {
    *reinterpret_cast<u32x4*>(lds + L_KS + tid * 16) = st[0];
    *reinterpret_cast<u32x4*>(lds + L_KZ + tid * 16) = st[1];
    *reinterpret_cast<u32x4*>(lds + L_VS + tid * 16) = st[2];
  }
  __syncthreads();

  char* const tw = lds + L_T + wave * TSZ + hh * 8 * TROW + ch * 8;
  const char* const tr = lds + L_T + wave * TSZ + c * TROW + g4 * 16;
  const bool lo_lane = (c >> 2) & 1;
  const unsigned sel = hh ? 0x03020c0cu : 0x01000c0cu;
  constexpr float SCALE = 0.088388347648318440f, LOG2E = 1.4426950408889634f;

  float m_run = -INFINITY, l_run = 0.f, z_run = 0.f;
  f32x16 acc[4];
#pragma unroll
  for (int cc = 0; cc < 4; ++cc)
#pragma unroll
    for (int r = 0; r < 16; ++r) acc[cc][r] = 0.f;

#pragma clang loop unroll(disable)
  for (int it = 0; it < NIT; ++it) {
    const unsigned tile = tile_of(it);
    const bool live = it + 1 < NIT;
    const unsigned so_nxt = (tokb + tile_of(it + 1) * 16) * 512u;
    const u32x2 sraw = *reinterpret_cast<const u32x2*>(lds + L_KS + (tile >> 2) * 256 + ch * 8);
    const u32x2 zraw = *reinterpret_cast<const u32x2*>(lds + L_KZ + (tile >> 2) * 256 + ch * 8);
    const u32x4 vsr = *reinterpret_cast<const u32x4*>(lds + L_VS + tile * 32 + (g4 & 1) * 16);
    const u32x4 vzr = *reinterpret_cast<const u32x4*>(lds + L_VZ + tile * 32 + (g4 & 1) * 16);
    const f32x2 s01 = {bf_lo(sraw[0]), bf_hi(sraw[0])}, s23 = {bf_lo(sraw[1]), bf_hi(sraw[1])};
    const f32x2 z01 = {bf_lo(zraw[0]), bf_hi(zraw[0])}, z23 = {bf_lo(zraw[1]), bf_hi(zraw[1])};
    SB();
#pragma unroll
    for (int n = 0; n < 8; ++n) {
      const i32x4 a = __builtin_bit_cast(i32x4, buf[n]);
      f32x2 t01 = {(float)a[0], (float)a[1]}, t23 = {(float)a[2], (float)a[3]};
      SB();
      { const int j = n + WIN; if (j < 16) issue(j, so_cur, true); else issue(j - 16, so_nxt, live); }
      SB();
      t01 -= z01; t23 -= z23;
      t01 = f32x2{rnd_bf16(t01[0]), rnd_bf16(t01[1])} * s01;
      t23 = f32x2{rnd_bf16(t23[0]), rnd_bf16(t23[1])} * s23;
      *reinterpret_cast<u32x2*>(tw + n * TROW) = u32x2{pk_bf16(t01[0], t01[1]), pk_bf16(t23[0], t23[1])};
    }
    asm volatile("" ::: "memory");
    f32x4 S = {0.f, 0.f, 0.f, 0.f};
#pragma unroll
    for (int m = 0; m < 4; ++m) S = mfma16(*reinterpret_cast<const u32x4*>(tr + m * 64), Bq[m], S);
    asm volatile("" ::: "memory");
    float y[4];
#pragma unroll
    for (int r = 0; r < 4; ++r) y[r] = rnd_bf16(rnd_bf16(S[r]) * SCALE) * LOG2E;
    const float m_new = fmaxf(m_run, xg_max(fmaxf(fmaxf(y[0], y[1]), fmaxf(y[2], y[3]))));
    const float alpha = __builtin_amdgcn_exp2f(m_run - m_new);
    m_run = m_new;
    float pv[4];
    float ls = 0.f, zs = 0.f;
#pragma unroll
    for (int r = 0; r < 4; ++r) {
      const float p = __builtin_amdgcn_exp2f(y[r] - m_new);
      ls += p;
      pv[r] = p * __builtin_bit_cast(float, __builtin_amdgcn_perm(vsr[r], vsr[r], sel));
      zs = fmaf(pv[r], __builtin_bit_cast(float, __builtin_amdgcn_perm(vzr[r], vzr[r], sel)), zs);
    }
    l_run = fmaf(l_run, alpha, ls);
    z_run = fmaf(z_run, alpha, zs);
    unsigned X[2];
#pragma unroll
    for (int u = 0; u < 2; ++u) {
      const unsigned h = pk_bf16(pv[2 * u], pv[2 * u + 1]);
      const unsigned l = pk_bf16(pv[2 * u] - bf_lo(h), pv[2 * u + 1] - bf_hi(h));
      X[u] = lo_lane ? l : h;
    }
    const auto w0 = __builtin_amdgcn_permlane16_swap(X[0], X[0], false, false);
    const auto w1 = __builtin_amdgcn_permlane16_swap(X[1], X[1], false, false);
    const u32x4 P = {w0[0], w1[0], w0[1], w1[1]};
#pragma unroll
    for (int r = 0; r < 4; ++r) {
      const float a = __builtin_bit_cast(float, __builtin_amdgcn_readlane(__builtin_bit_cast(int, alpha), r));
#pragma unroll
      for (int cc = 0; cc < 4; ++cc) acc[cc][r] *= a;
    }
    SB();
    u32x4 Bv[4];
#pragma unroll
    for (int u = 0; u < 4; ++u) {
      const i32x4 a = __builtin_bit_cast(i32x4, buf[8 + 2 * u]), b = __builtin_bit_cast(i32x4, buf[9 + 2 * u]);
#pragma unroll
      for (int cc = 0; cc < 4; ++cc) Bv[cc][u] = pk_bf16((float)a[cc], (float)b[cc]);
      SB();
#pragma unroll
      for (int e = 0; e < 2; ++e) { const int j = 8 + 2 * u + e + WIN; if (j < 16) issue(j, so_cur, true); else issue(j - 16, so_nxt, live); }
      SB();
    }
#pragma unroll
    for (int cc = 0; cc < 4; ++cc) acc[cc] = mfma32(P, Bv[cc], acc[cc]);
    so_cur = so_nxt;
  }

  l_run = xg_sum(l_run);
  z_run = xg_sum(z_run);
  float* rec = reinterpret_cast<float*>(lds + L_T + wave * TSZ);
#pragma unroll
  for (int r = 0; r < 4; ++r)
    *reinterpret_cast<f32x4*>(rec + (hh * 4 + r) * D + 4 * ch) = f32x4{acc[0][r], acc[1][r], acc[2][r], acc[3][r]};
  if (lane < 4) { rec[1024 + lane] = m_run; rec[1028 + lane] = l_run; rec[1032 + lane] = z_run; }
  __syncthreads();
  for (int idx = tid; idx < 4 * D; idx += NTHR) {
    const int h = idx >> 7, d = idx & 127;
    float M = -INFINITY;
#pragma unroll
    for (int w = 0; w < WAVES; ++w) M = fmaxf(M, reinterpret_cast<const float*>(lds + L_T + w * TSZ)[1024 + h]);
    float L = 0.f, o = 0.f;
#pragma unroll
    for (int w = 0; w < WAVES; ++w) {
      const float* rw = reinterpret_cast<const float*>(lds + L_T + w * TSZ);
      const float f = __builtin_amdgcn_exp2f(rw[1024 + h] - M);
      L = fmaf(f, rw[1028 + h], L);
      o = fmaf(f, (rw[h * D + d] + rw[(4 + h) * D + d]) - rw[1032 + h], o);
    }
    const size_t slot = ((size_t)bh * REP + h) * NSPLIT + split;
    po[slot * D + d] = o;
    if (d == 0) { pml[2 * slot] = M; pml[2 * slot + 1] = L; }
  }
}

__global__ void __launch_bounds__(128)
kv_combine(const float* __restrict__ po, const float* __restrict__ pml, __hip_bfloat16* __restrict__ out) {
  const int row = blockIdx.x, d = threadIdx.x;
  float M = -INFINITY;
#pragma unroll
  for (int s = 0; s < NSPLIT; ++s) M = fmaxf(M, pml[2 * (row * NSPLIT + s)]);
  float L = 0.f, o = 0.f;
#pragma unroll
  for (int s = 0; s < NSPLIT; ++s) {
    const float f = __builtin_amdgcn_exp2f(pml[2 * (row * NSPLIT + s)] - M);
    L = fmaf(f, pml[2 * (row * NSPLIT + s) + 1], L);
    o = fmaf(f, po[(size_t)(row * NSPLIT + s) * D + d], o);
  }
  out[row * D + d] = __float2bfloat16(o / L);
}

extern "C" void kernel_launch(void* const* d_in, const int* in_sizes, int n_in, void* d_out, int out_size,
                              void* d_ws, size_t ws_size, hipStream_t stream) {
  const uint16_t* q = (const uint16_t*)d_in[0];
  const int* qk = (const int*)d_in[1];
  const int* qv = (const int*)d_in[2];
  const uint16_t* ks = (const uint16_t*)d_in[3];
  const uint16_t* kz = (const uint16_t*)d_in[4];
  const uint16_t* vs = (const uint16_t*)d_in[5];
  const uint16_t* vz = (const uint16_t*)d_in[6];
  float* pml = (float*)d_ws;
  float* po = (float*)((char*)d_ws + 65536);
  kv_split<<<NWG, NTHR, 0, stream>>>(q, qk, qv, ks, kz, vs, vz, po, pml);
  kv_combine<<<B_ * HQ, 128, 0, stream>>>(po, pml, (__hip_bfloat16*)d_out);
}
```

```cpp
#define PK1 0
#define PK2 2
#define PV1 2
#define PV2 2
#include <hip/hip_runtime.h>
#include <hip/hip_bf16.h>
#include <stdint.h>

#define WAVES 4
#define WIN 8
#define ROT 1

constexpr int B_ = 4, HQ = 32, HKV = 8, SKV = 8192, D = 128, GRP = 64, NG = SKV / GRP, REP = HQ / HKV;
constexpr int NBH = B_ * HKV;
constexpr int NSPLIT = 8, NWG = NBH * NSPLIT;
constexpr int TOKWG = SKV / NSPLIT;
constexpr int NTHR = WAVES * 64;
constexpr int NIT = TOKWG / 16 / WAVES;
constexpr unsigned KVBYTES = (unsigned)B_ * HKV * SKV * D * 4u;

constexpr int L_KS = 0, L_KZ = 4096, L_VS = 8192, L_VZ = 10240, L_T = 12288;
constexpr int TROW = 272, TSZ = 16 * TROW;
constexpr int L_END = L_T + WAVES * TSZ;

typedef int i32x4 __attribute__((ext_vector_type(4)));
typedef unsigned u32x4 __attribute__((ext_vector_type(4)));
typedef unsigned u32x2 __attribute__((ext_vector_type(2)));
typedef float f32x4 __attribute__((ext_vector_type(4)));
typedef float f32x2 __attribute__((ext_vector_type(2)));
typedef float f32x16 __attribute__((ext_vector_type(16)));
typedef __bf16 bf16x8 __attribute__((ext_vector_type(8)));
typedef __bf16 bf16x2 __attribute__((ext_vector_type(2)));

__device__ __forceinline__ unsigned pk_bf16(float lo, float hi) {
  f32x2 v = {lo, hi};
  return __builtin_bit_cast(unsigned, __builtin_convertvector(v, bf16x2));
}
__device__ __forceinline__ float rnd_bf16(float x) {
  return __builtin_bit_cast(float, pk_bf16(0.f, x));
}
__device__ __forceinline__ float bf_lo(unsigned u) { return __builtin_bit_cast(float, u << 16); }
__device__ __forceinline__ float bf_hi(unsigned u) { return __builtin_bit_cast(float, u & 0xffff0000u); }
__device__ __forceinline__ f32x4 mfma16(u32x4 a, u32x4 b, f32x4 c) {
  return __builtin_amdgcn_mfma_f32_16x16x32_bf16(__builtin_bit_cast(bf16x8, a), __builtin_bit_cast(bf16x8, b), c, 0, 0, 0);
}
__device__ __forceinline__ f32x16 mfma32(u32x4 a, u32x4 b, f32x16 c) {
  return __builtin_amdgcn_mfma_f32_32x32x16_bf16(__builtin_bit_cast(bf16x8, a), __builtin_bit_cast(bf16x8, b), c, 0, 0, 0);
}
__device__ __forceinline__ float xg_max(float x) {
  auto s = __builtin_amdgcn_permlane16_swap(__float_as_uint(x), __float_as_uint(x), false, false);
  x = fmaxf(__uint_as_float(s[0]), __uint_as_float(s[1]));
  auto t = __builtin_amdgcn_permlane32_swap(__float_as_uint(x), __float_as_uint(x), false, false);
  return fmaxf(__uint_as_float(t[0]), __uint_as_float(t[1]));
}
__device__ __forceinline__ float xg_sum(float x) {
  auto s = __builtin_amdgcn_permlane16_swap(__float_as_uint(x), __float_as_uint(x), false, false);
  x = __uint_as_float(s[0]) + __uint_as_float(s[1]);
  auto t = __builtin_amdgcn_permlane32_swap(__float_as_uint(x), __float_as_uint(x), false, false);
  return __uint_as_float(t[0]) + __uint_as_float(t[1]);
}
#define SB() __builtin_amdgcn_sched_barrier(0)
constexpr unsigned WS_EP = 0, WS_REC = 16384, RECSZ = 1024 + 64;
constexpr unsigned WS_END = WS_REC + NBH * REP * NSPLIT * RECSZ;
#define ST(k)

template <int AK, int AV> __device__ __forceinline__ void
kv_body(char* lds, const uint16_t* __restrict__ q, const int* __restrict__ qk, const int* __restrict__ qv,
         const uint16_t* __restrict__ ks, const uint16_t* __restrict__ kz,
         const uint16_t* __restrict__ vs, const uint16_t* __restrict__ vz,
         unsigned* ws, unsigned* out) {
  ST(0);
  const int bid = (blockIdx.x & 7) * (NWG / 8) + (blockIdx.x >> 3);
  const int bh = bid / NSPLIT, split = bid % NSPLIT;
  const int tid = threadIdx.x, lane = tid & 63;
  const int wave = __builtin_amdgcn_readfirstlane(tid >> 6);
  const int c = lane & 15, g4 = lane >> 4, hh = lane >> 5, ch = lane & 31;
  const unsigned tokb = (unsigned)bh * SKV + (unsigned)split * TOKWG;
  const unsigned rot = ROT ? (blockIdx.x >> 3) : 0;

  const __amdgpu_buffer_rsrc_t rws = __builtin_amdgcn_make_buffer_rsrc((void*)ws, 0, (int)WS_END, 0x00020000);
  const unsigned row = (unsigned)bh * REP + (wave & 3);
  const unsigned ep = __builtin_amdgcn_raw_buffer_load_b32(rws, 0, WS_EP + row * 64, 16);
  u32x4 st[3];
  if (tid < 256) {
    const size_t go = ((size_t)bh * NG + split * (TOKWG / GRP)) * D;
    st[0] = **reinterpret_cast<const u32x4(*)[1]>(ks + go + tid * 8);
    st[1] = **reinterpret_cast<const u32x4(*)[1]>(kz + go + tid * 8);
    st[2] = **reinterpret_cast<const u32x4(*)[1]>((tid < 128 ? vs : vz) + tokb + (tid & 127) * 8);
  }
  u32x4 Bq[4];
#pragma unroll
  for (int m = 0; m < 4; ++m)
    Bq[m] = *reinterpret_cast<const u32x4*>(q + ((size_t)bh * REP + (c & 3)) * D + 32 * m + 8 * g4);
  SB();

  const unsigned vo0 = lane * 16, vo1 = lane * 16 + 4096;
  u32x4 buf[16];
  auto tile_of = [&](int it) -> unsigned { return (((unsigned)it + rot) & (NIT - 1)) * WAVES + wave; };
  auto issue = [&](int slot, const unsigned so, const bool live) __attribute__((always_inline)) {
    __amdgpu_buffer_rsrc_t r = __builtin_amdgcn_make_buffer_rsrc((void*)(slot < 8 ? qk : qv), 0, live ? (int)KVBYTES : 0, 0x00020000);
    const int n = slot & 7;
    if (slot < 8) buf[slot] = __builtin_amdgcn_raw_buffer_load_b128(r, (n < 4 ? vo0 : vo1) + (n & 3) * 1024, so, AK); else buf[slot] = __builtin_amdgcn_raw_buffer_load_b128(r, (n < 4 ? vo0 : vo1) + (n & 3) * 1024, so, AV);
  };
  unsigned so_cur = (tokb + tile_of(0) * 16) * 512u;
#pragma unroll
  for (int i = 0; i < WIN; ++i) issue(i, so_cur, true);
  SB();
  if (tid < 256) {
    *reinterpret_cast<u32x4*>(lds + L_KS + tid * 16) = st[0];
    *reinterpret_cast<u32x4*>(lds + L_KZ + tid * 16) = st[1];
    *reinterpret_cast<u32x4*>(lds + L_VS + tid * 16) = st[2];
  }
  const unsigned tag = __builtin_amdgcn_readfirstlane(ep) + 0x9E3779B9u;
  __syncthreads();
  ST(1);

  char* const tw = lds + L_T + wave * TSZ + hh * 8 * TROW + ch * 8;
  const char* const tr = lds + L_T + wave * TSZ + c * TROW + g4 * 16;
  const bool lo_lane = (c >> 2) & 1;
  const unsigned sel = hh ? 0x03020c0cu : 0x01000c0cu;
  constexpr float SCALE = 0.088388347648318440f, LOG2E = 1.4426950408889634f;

  float m_run = -INFINITY, l_run = 0.f, z_run = 0.f;
  f32x16 acc[4];
#pragma unroll
  for (int cc = 0; cc < 4; ++cc)
#pragma unroll
    for (int r = 0; r < 16; ++r) acc[cc][r] = 0.f;

#pragma clang loop unroll(disable)
  for (int it = 0; it < NIT; ++it) {
    const unsigned tile = tile_of(it);
    const bool live = it + 1 < NIT;
    const unsigned so_nxt = (tokb + tile_of(it + 1) * 16) * 512u;
    const u32x2 sraw = *reinterpret_cast<const u32x2*>(lds + L_KS + (tile >> 2) * 256 + ch * 8);
    const u32x2 zraw = *reinterpret_cast<const u32x2*>(lds + L_KZ + (tile >> 2) * 256 + ch * 8);
    const u32x4 vsr = *reinterpret_cast<const u32x4*>(lds + L_VS + tile * 32 + (g4 & 1) * 16);
    const u32x4 vzr = *reinterpret_cast<const u32x4*>(lds + L_VZ + tile * 32 + (g4 & 1) * 16);
    const f32x2 s01 = {bf_lo(sraw[0]), bf_hi(sraw[0])}, s23 = {bf_lo(sraw[1]), bf_hi(sraw[1])};
    const f32x2 z01 = {bf_lo(zraw[0]), bf_hi(zraw[0])}, z23 = {bf_lo(zraw[1]), bf_hi(zraw[1])};
    SB();
#pragma unroll
    for (int n = 0; n < 8; ++n) {
      const i32x4 a = __builtin_bit_cast(i32x4, buf[n]);
      f32x2 t01 = {(float)a[0], (float)a[1]}, t23 = {(float)a[2], (float)a[3]};
      SB();
      { const int j = n + WIN; if (j < 16) issue(j, so_cur, true); else issue(j - 16, so_nxt, live); }
      SB();
      t01 -= z01; t23 -= z23;
      t01 = f32x2{rnd_bf16(t01[0]), rnd_bf16(t01[1])} * s01;
      t23 = f32x2{rnd_bf16(t23[0]), rnd_bf16(t23[1])} * s23;
      *reinterpret_cast<u32x2*>(tw + n * TROW) = u32x2{pk_bf16(t01[0], t01[1]), pk_bf16(t23[0], t23[1])};
    }
    asm volatile("" ::: "memory");
    f32x4 S = {0.f, 0.f, 0.f, 0.f};
#pragma unroll
    for (int m = 0; m < 4; ++m) S = mfma16(*reinterpret_cast<const u32x4*>(tr + m * 64), Bq[m], S);
    asm volatile("" ::: "memory");
    float y[4];
#pragma unroll
    for (int r = 0; r < 4; ++r) y[r] = rnd_bf16(rnd_bf16(S[r]) * SCALE) * LOG2E;
    const float m_new = fmaxf(m_run, xg_max(fmaxf(fmaxf(y[0], y[1]), fmaxf(y[2], y[3]))));
    const float alpha = __builtin_amdgcn_exp2f(m_run - m_new);
    m_run = m_new;
    float pv[4];
    float ls = 0.f, zs = 0.f;
#pragma unroll
    for (int r = 0; r < 4; ++r) {
      const float p = __builtin_amdgcn_exp2f(y[r] - m_new);
      ls += p;
      pv[r] = p * __builtin_bit_cast(float, __builtin_amdgcn_perm(vsr[r], vsr[r], sel));
      zs = fmaf(pv[r], __builtin_bit_cast(float, __builtin_amdgcn_perm(vzr[r], vzr[r], sel)), zs);
    }
    l_run = fmaf(l_run, alpha, ls);
    z_run = fmaf(z_run, alpha, zs);
    unsigned X[2];
#pragma unroll
    for (int u = 0; u < 2; ++u) {
      const unsigned h = pk_bf16(pv[2 * u], pv[2 * u + 1]);
      const unsigned l = pk_bf16(pv[2 * u] - bf_lo(h), pv[2 * u + 1] - bf_hi(h));
      X[u] = lo_lane ? l : h;
    }
    const auto w0 = __builtin_amdgcn_permlane16_swap(X[0], X[0], false, false);
    const auto w1 = __builtin_amdgcn_permlane16_swap(X[1], X[1], false, false);
    const u32x4 P = {w0[0], w1[0], w0[1], w1[1]};
#pragma unroll
    for (int r = 0; r < 4; ++r) {
      const float a = __builtin_bit_cast(float, __builtin_amdgcn_readlane(__builtin_bit_cast(int, alpha), r));
#pragma unroll
      for (int cc = 0; cc < 4; ++cc) acc[cc][r] *= a;
    }
    SB();
    u32x4 Bv[4];
#pragma unroll
    for (int u = 0; u < 4; ++u) {
      const i32x4 a = __builtin_bit_cast(i32x4, buf[8 + 2 * u]), b = __builtin_bit_cast(i32x4, buf[9 + 2 * u]);
#pragma unroll
      for (int cc = 0; cc < 4; ++cc) Bv[cc][u] = pk_bf16((float)a[cc], (float)b[cc]);
      SB();
#pragma unroll
      for (int e = 0; e < 2; ++e) { const int j = 8 + 2 * u + e + WIN; if (j < 16) issue(j, so_cur, true); else issue(j - 16, so_nxt, live); }
      SB();
    }
#pragma unroll
    for (int cc = 0; cc < 4; ++cc) acc[cc] = mfma32(P, Bv[cc], acc[cc]);
    so_cur = so_nxt;
  }
  ST(3);

  l_run = xg_sum(l_run);
  z_run = xg_sum(z_run);
  float* rec = reinterpret_cast<float*>(lds + L_T + wave * TSZ);
#pragma unroll
  for (int r = 0; r < 4; ++r)
    *reinterpret_cast<f32x4*>(rec + (hh * 4 + r) * D + 4 * ch) = f32x4{acc[0][r], acc[1][r], acc[2][r], acc[3][r]};
  if (lane < 4) { rec[1024 + lane] = m_run; rec[1028 + lane] = l_run; rec[1032 + lane] = z_run; }
  __syncthreads();
  ST(4);
  if (wave < 4) {
    const int h = wave;
    float M = -INFINITY;
#pragma unroll
    for (int w = 0; w < WAVES; ++w) M = fmaxf(M, reinterpret_cast<const float*>(lds + L_T + w * TSZ)[1024 + h]);
    float L = 0.f, o0 = 0.f, o1 = 0.f;
#pragma unroll
    for (int w = 0; w < WAVES; ++w) {
      const float* rw = reinterpret_cast<const float*>(lds + L_T + w * TSZ);
      const float f = __builtin_amdgcn_exp2f(rw[1024 + h] - M);
      const f32x2 hi = *reinterpret_cast<const f32x2*>(rw + h * D + 2 * lane), lo = *reinterpret_cast<const f32x2*>(rw + (4 + h) * D + 2 * lane);
      const float z = rw[1032 + h];
      L = fmaf(f, rw[1028 + h], L);
      o0 = fmaf(f, (hi[0] + lo[0]) - z, o0);
      o1 = fmaf(f, (hi[1] + lo[1]) - z, o1);
    }
    const unsigned rbase = WS_REC + row * NSPLIT * RECSZ;
    __builtin_amdgcn_raw_buffer_store_b128(u32x4{__float_as_uint(o0), tag, __float_as_uint(o1), tag}, rws, lane * 16, rbase + split * RECSZ, 16);
    if (lane == 0) __builtin_amdgcn_raw_buffer_store_b128(u32x4{__float_as_uint(M), tag, __float_as_uint(L), tag}, rws, 1024, rbase + split * RECSZ, 16);
    ST(5);
#pragma clang loop unroll(disable)
    for (int look = 0; look < 2; ++look) {
      u32x4 g[NSPLIT];
      unsigned rb = rbase;
      asm volatile("" : "+s"(rb) :: "memory");
      u32x4 ml = __builtin_amdgcn_raw_buffer_load_b128(rws, (lane & 7) * RECSZ + 1024, rb, 16);
#pragma unroll
      for (int s = 0; s < NSPLIT; ++s) g[s] = __builtin_amdgcn_raw_buffer_load_b128(rws, lane * 16, rb + s * RECSZ, 16);
      asm volatile("s_waitcnt vmcnt(0)" ::: "memory");
      if ((lane & 7) == split) ml = u32x4{__float_as_uint(M), tag, __float_as_uint(L), tag};
      bool ok = ml[1] == tag && ml[3] == tag;
#pragma unroll
      for (int s = 0; s < NSPLIT; ++s) {
        if (s == split) g[s] = u32x4{__float_as_uint(o0), tag, __float_as_uint(o1), tag};
        ok = ok && g[s][1] == tag && g[s][3] == tag;
      }
      if (__all(ok)) {
        float ms[NSPLIT], Mx = -INFINITY;
#pragma unroll
        for (int s = 0; s < NSPLIT; ++s) { ms[s] = __uint_as_float(__builtin_amdgcn_readlane(ml[0], s)); Mx = fmaxf(Mx, ms[s]); }
        float Lx = 0.f, a0 = 0.f, a1 = 0.f;
#pragma unroll
        for (int s = 0; s < NSPLIT; ++s) {
          const float f = __builtin_amdgcn_exp2f(ms[s] - Mx);
          Lx = fmaf(f, __uint_as_float(__builtin_amdgcn_readlane(ml[2], s)), Lx);
          a0 = fmaf(f, __uint_as_float(g[s][0]), a0);
          a1 = fmaf(f, __uint_as_float(g[s][2]), a1);
        }
        out[row * (D / 2) + lane] = pk_bf16(a0 / Lx, a1 / Lx);
        if (lane == 0) __builtin_amdgcn_raw_buffer_store_b32(tag, rws, 0, WS_EP + row * 64, 16);
        ST(7);
        break;
      }
    }
    ST(6);
  }
}

__global__ void __launch_bounds__(NTHR, 2)
kv_split(const uint16_t* __restrict__ q, const int* __restrict__ qk, const int* __restrict__ qv,
         const uint16_t* __restrict__ ks, const uint16_t* __restrict__ kz,
         const uint16_t* __restrict__ vs, const uint16_t* __restrict__ vz,
         unsigned* ws, unsigned* out) {
  __shared__ __attribute__((aligned(16))) char lds[L_END];
  const bool second = (blockIdx.x & 7) >= 4;
  if (second) kv_body<PK2, PV2>(lds, q, qk, qv, ks, kz, vs, vz, ws, out);
  else kv_body<PK1, PV1>(lds, q, qk, qv, ks, kz, vs, vz, ws, out);
}

extern "C" void kernel_launch(void* const* d_in, const int* in_sizes, int n_in, void* d_out, int out_size,
                              void* d_ws, size_t ws_size, hipStream_t stream) {
  const uint16_t* q = (const uint16_t*)d_in[0];
  const int* qk = (const int*)d_in[1];
  const int* qv = (const int*)d_in[2];
  const uint16_t* ks = (const uint16_t*)d_in[3];
  const uint16_t* kz = (const uint16_t*)d_in[4];
  const uint16_t* vs = (const uint16_t*)d_in[5];
  const uint16_t* vz = (const uint16_t*)d_in[6];
  kv_split<<<NWG, NTHR, 0, stream>>>(q, qk, qv, ks, kz, vs, vz, (unsigned*)d_ws, (unsigned*)d_out);
}
```

```cpp
#define STAMP
#define QMODE 0
#define QK 6
#define AMP 0
#include <hip/hip_runtime.h>
#include <hip/hip_bf16.h>
#include <stdint.h>

#define WAVES 4
#define WIN 8
#define ROT 1

constexpr int B_ = 4, HQ = 32, HKV = 8, SKV = 8192, D = 128, GRP = 64, NG = SKV / GRP, REP = HQ / HKV;
constexpr int NBH = B_ * HKV;
constexpr int NSPLIT = 8, NWG = NBH * NSPLIT;
constexpr int TOKWG = SKV / NSPLIT;
constexpr int NTHR = WAVES * 64;
constexpr int NIT = TOKWG / 16 / WAVES;
constexpr unsigned KVBYTES = (unsigned)B_ * HKV * SKV * D * 4u;

constexpr int L_KS = 0, L_KZ = 4096, L_VS = 8192, L_VZ = 10240, L_T = 12288;
constexpr int TROW = 272, TSZ = 16 * TROW;
constexpr int L_END = L_T + WAVES * TSZ;

typedef int i32x4 __attribute__((ext_vector_type(4)));
typedef unsigned u32x4 __attribute__((ext_vector_type(4)));
typedef unsigned u32x2 __attribute__((ext_vector_type(2)));
typedef float f32x4 __attribute__((ext_vector_type(4)));
typedef float f32x2 __attribute__((ext_vector_type(2)));
typedef float f32x16 __attribute__((ext_vector_type(16)));
typedef __bf16 bf16x8 __attribute__((ext_vector_type(8)));
typedef __bf16 bf16x2 __attribute__((ext_vector_type(2)));

__device__ __forceinline__ unsigned pk_bf16(float lo, float hi) {
  f32x2 v = {lo, hi};
  return __builtin_bit_cast(unsigned, __builtin_convertvector(v, bf16x2));
}
__device__ __forceinline__ float rnd_bf16(float x) {
  return __builtin_bit_cast(float, pk_bf16(0.f, x));
}
__device__ __forceinline__ float bf_lo(unsigned u) { return __builtin_bit_cast(float, u << 16); }
__device__ __forceinline__ float bf_hi(unsigned u) { return __builtin_bit_cast(float, u & 0xffff0000u); }
__device__ __forceinline__ f32x4 mfma16(u32x4 a, u32x4 b, f32x4 c) {
  return __builtin_amdgcn_mfma_f32_16x16x32_bf16(__builtin_bit_cast(bf16x8, a), __builtin_bit_cast(bf16x8, b), c, 0, 0, 0);
}
__device__ __forceinline__ f32x16 mfma32(u32x4 a, u32x4 b, f32x16 c) {
  return __builtin_amdgcn_mfma_f32_32x32x16_bf16(__builtin_bit_cast(bf16x8, a), __builtin_bit_cast(bf16x8, b), c, 0, 0, 0);
}
__device__ __forceinline__ float xg_max(float x) {
  auto s = __builtin_amdgcn_permlane16_swap(__float_as_uint(x), __float_as_uint(x), false, false);
  x = fmaxf(__uint_as_float(s[0]), __uint_as_float(s[1]));
  auto t = __builtin_amdgcn_permlane32_swap(__float_as_uint(x), __float_as_uint(x), false, false);
  return fmaxf(__uint_as_float(t[0]), __uint_as_float(t[1]));
}
__device__ __forceinline__ float xg_sum(float x) {
  auto s = __builtin_amdgcn_permlane16_swap(__float_as_uint(x), __float_as_uint(x), false, false);
  x = __uint_as_float(s[0]) + __uint_as_float(s[1]);
  auto t = __builtin_amdgcn_permlane32_swap(__float_as_uint(x), __float_as_uint(x), false, false);
  return __uint_as_float(t[0]) + __uint_as_float(t[1]);
}
#define SB() __builtin_amdgcn_sched_barrier(0)
constexpr unsigned WS_EP = 0, WS_REC = 16384, RECSZ = 1024 + 64;
constexpr unsigned WS_END = WS_REC + NBH * REP * NSPLIT * RECSZ;
#define NSTAMP 8
#define STAMP_WAVES (NWG * WAVES)
#define STAMP_OFF (4u << 20)
typedef unsigned long long u64s;
__device__ __forceinline__ u64s rt() { u64s t; asm volatile("s_memrealtime %0\n\ts_waitcnt lgkmcnt(0)" : "=s"(t) :: "memory"); return t; }
#define ST(k) do { SB(); stamp[k] = rt(); SB(); } while (0)

__global__ void __launch_bounds__(NTHR, 2)
kv_split(const uint16_t* __restrict__ q, const int* __restrict__ qk, const int* __restrict__ qv,
         const uint16_t* __restrict__ ks, const uint16_t* __restrict__ kz,
         const uint16_t* __restrict__ vs, const uint16_t* __restrict__ vz,
         unsigned* ws, unsigned* out) {
  __shared__ __attribute__((aligned(16))) char lds[L_END];
  u64s stamp[NSTAMP] = {0, 0, 0, 0, 0, 0, 0, 0};
  ST(0);
  const unsigned long long cyc0 = __builtin_amdgcn_s_memtime();
  const int bid = (blockIdx.x & 7) * (NWG / 8) + (blockIdx.x >> 3);
  const int bh = bid / NSPLIT, split = bid % NSPLIT;
  const int tid = threadIdx.x, lane = tid & 63;
  const int wave = __builtin_amdgcn_readfirstlane(tid >> 6);
  const int c = lane & 15, g4 = lane >> 4, hh = lane >> 5, ch = lane & 31;
  const unsigned tokb = (unsigned)bh * SKV + (unsigned)split * TOKWG;
  const unsigned rot = ROT ? (blockIdx.x >> 3) : 0;

  const __amdgpu_buffer_rsrc_t rws = __builtin_amdgcn_make_buffer_rsrc((void*)ws, 0, (int)WS_END, 0x00020000);
  const unsigned row = (unsigned)bh * REP + (wave & 3);
  const unsigned ep = __builtin_amdgcn_raw_buffer_load_b32(rws, 0, WS_EP + row * 64, 16);
  u32x4 st[3];
  if (tid < 256) {
    const size_t go = ((size_t)bh * NG + split * (TOKWG / GRP)) * D;
    st[0] = **reinterpret_cast<const u32x4(*)[1]>(ks + go + tid * 8);
    st[1] = **reinterpret_cast<const u32x4(*)[1]>(kz + go + tid * 8);
    st[2] = **reinterpret_cast<const u32x4(*)[1]>((tid < 128 ? vs : vz) + tokb + (tid & 127) * 8);
  }
  u32x4 Bq[4];
#pragma unroll
  for (int m = 0; m < 4; ++m)
    Bq[m] = *reinterpret_cast<const u32x4*>(q + ((size_t)bh * REP + (c & 3)) * D + 32 * m + 8 * g4);
  SB();

  const unsigned vo0 = lane * 16, vo1 = lane * 16 + 4096;
  u32x4 buf[16];
  auto tile_of = [&](int it) -> unsigned { return (((unsigned)it + rot) & (NIT - 1)) * WAVES + wave; };
  auto issue = [&](int slot, const unsigned so, const bool live) __attribute__((always_inline)) {
    __amdgpu_buffer_rsrc_t r = __builtin_amdgcn_make_buffer_rsrc((void*)(slot < 8 ? qk : qv), 0, live ? (int)KVBYTES : 0, 0x00020000);
    const int n = slot & 7;
    buf[slot] = __builtin_amdgcn_raw_buffer_load_b128(r, (n < 4 ? vo0 : vo1) + (n & 3) * 1024, so, 2);
  };
  unsigned so_cur = (tokb + tile_of(0) * 16) * 512u;
#pragma unroll
  for (int i = 0; i < WIN; ++i) issue(i, so_cur, true);
  SB();
  if (tid < 256) {
    *reinterpret_cast<u32x4*>(lds + L_KS + tid * 16) = st[0];
    *reinterpret_cast<u32x4*>(lds + L_KZ + tid * 16) = st[1];
    *reinterpret_cast<u32x4*>(lds + L_VS + tid * 16) = st[2];
  }
  const unsigned tag = __builtin_amdgcn_readfirstlane(ep) + 0x9E3779B9u;
  __syncthreads();
  ST(1);

  char* const tw = lds + L_T + wave * TSZ + hh * 8 * TROW + ch * 8;
  const char* const tr = lds + L_T + wave * TSZ + c * TROW + g4 * 16;
  const bool lo_lane = (c >> 2) & 1;
  const unsigned sel = hh ? 0x03020c0cu : 0x01000c0cu;
  constexpr float SCALE = 0.088388347648318440f, LOG2E = 1.4426950408889634f;

  float m_run = -INFINITY, l_run = 0.f, z_run = 0.f;
  f32x16 acc[4];
#pragma unroll
  for (int cc = 0; cc < 4; ++cc)
#pragma unroll
    for (int r = 0; r < 16; ++r) acc[cc][r] = 0.f;

#pragma clang loop unroll(disable)
  for (int it = 0; it < NIT; ++it) {
    const unsigned tile = tile_of(it);
    const bool live = it + 1 < NIT;
    const unsigned so_nxt = (tokb + tile_of(it + 1) * 16) * 512u;
    const u32x2 sraw = *reinterpret_cast<const u32x2*>(lds + L_KS + (tile >> 2) * 256 + ch * 8);
    const u32x2 zraw = *reinterpret_cast<const u32x2*>(lds + L_KZ + (tile >> 2) * 256 + ch * 8);
    const u32x4 vsr = *reinterpret_cast<const u32x4*>(lds + L_VS + tile * 32 + (g4 & 1) * 16);
    const u32x4 vzr = *reinterpret_cast<const u32x4*>(lds + L_VZ + tile * 32 + (g4 & 1) * 16);
    const f32x2 s01 = {bf_lo(sraw[0]), bf_hi(sraw[0])}, s23 = {bf_lo(sraw[1]), bf_hi(sraw[1])};
    const f32x2 z01 = {bf_lo(zraw[0]), bf_hi(zraw[0])}, z23 = {bf_lo(zraw[1]), bf_hi(zraw[1])};
    SB();
#pragma unroll
    for (int n = 0; n < 8; ++n) {
      const i32x4 a = __builtin_bit_cast(i32x4, buf[n]);
      f32x2 t01 = {(float)a[0], (float)a[1]}, t23 = {(float)a[2], (float)a[3]};
      SB();
      { const int j = n + WIN; if (j < 16) issue(j, so_cur, true); else issue(j - 16, so_nxt, live); }
      SB();
      t01 -= z01; t23 -= z23;
      t01 = f32x2{rnd_bf16(t01[0]), rnd_bf16(t01[1])} * s01;
      t23 = f32x2{rnd_bf16(t23[0]), rnd_bf16(t23[1])} * s23;
      *reinterpret_cast<u32x2*>(tw + n * TROW) = u32x2{pk_bf16(t01[0], t01[1]), pk_bf16(t23[0], t23[1])};
    }
    asm volatile("" ::: "memory");
    f32x4 S = {0.f, 0.f, 0.f, 0.f};
#pragma unroll
    for (int m = 0; m < 4; ++m) S = mfma16(*reinterpret_cast<const u32x4*>(tr + m * 64), Bq[m], S);
    asm volatile("" ::: "memory");
    float y[4];
#pragma unroll
    for (int r = 0; r < 4; ++r) y[r] = rnd_bf16(rnd_bf16(S[r]) * SCALE) * LOG2E;
    const float m_new = fmaxf(m_run, xg_max(fmaxf(fmaxf(y[0], y[1]), fmaxf(y[2], y[3]))));
    const float alpha = __builtin_amdgcn_exp2f(m_run - m_new);
    m_run = m_new;
    float pv[4];
    float ls = 0.f, zs = 0.f;
#pragma unroll
    for (int r = 0; r < 4; ++r) {
      const float p = __builtin_amdgcn_exp2f(y[r] - m_new);
      ls += p;
      pv[r] = p * __builtin_bit_cast(float, __builtin_amdgcn_perm(vsr[r], vsr[r], sel));
      zs = fmaf(pv[r], __builtin_bit_cast(float, __builtin_amdgcn_perm(vzr[r], vzr[r], sel)), zs);
    }
    l_run = fmaf(l_run, alpha, ls);
    z_run = fmaf(z_run, alpha, zs);
    unsigned X[2];
#pragma unroll
    for (int u = 0; u < 2; ++u) {
      const unsigned h = pk_bf16(pv[2 * u], pv[2 * u + 1]);
      const unsigned l = pk_bf16(pv[2 * u] - bf_lo(h), pv[2 * u + 1] - bf_hi(h));
      X[u] = lo_lane ? l : h;
    }
    const auto w0 = __builtin_amdgcn_permlane16_swap(X[0], X[0], false, false);
    const auto w1 = __builtin_amdgcn_permlane16_swap(X[1], X[1], false, false);
    const u32x4 P = {w0[0], w1[0], w0[1], w1[1]};
#pragma unroll
    for (int r = 0; r < 4; ++r) {
      const float a = __builtin_bit_cast(float, __builtin_amdgcn_readlane(__builtin_bit_cast(int, alpha), r));
#pragma unroll
      for (int cc = 0; cc < 4; ++cc) acc[cc][r] *= a;
    }
    SB();
    u32x4 Bv[4];
#pragma unroll
    for (int u = 0; u < 4; ++u) {
      const i32x4 a = __builtin_bit_cast(i32x4, buf[8 + 2 * u]), b = __builtin_bit_cast(i32x4, buf[9 + 2 * u]);
#pragma unroll
      for (int cc = 0; cc < 4; ++cc) Bv[cc][u] = pk_bf16((float)a[cc], (float)b[cc]);
      SB();
#pragma unroll
      for (int e = 0; e < 2; ++e) { const int j = 8 + 2 * u + e + WIN; if (j < 16) issue(j, so_cur, true); else issue(j - 16, so_nxt, live); }
      SB();
    }
#pragma unroll
    for (int cc = 0; cc < 4; ++cc) acc[cc] = mfma32(P, Bv[cc], acc[cc]);
    so_cur = so_nxt;
    if (it == 0) ST(2);
  }
  ST(3);
  stamp[7] = __builtin_amdgcn_s_memtime() - cyc0;

  l_run = xg_sum(l_run);
  z_run = xg_sum(z_run);
  float* rec = reinterpret_cast<float*>(lds + L_T + wave * TSZ);
#pragma unroll
  for (int r = 0; r < 4; ++r)
    *reinterpret_cast<f32x4*>(rec + (hh * 4 + r) * D + 4 * ch) = f32x4{acc[0][r], acc[1][r], acc[2][r], acc[3][r]};
  if (lane < 4) { rec[1024 + lane] = m_run; rec[1028 + lane] = l_run; rec[1032 + lane] = z_run; }
  __syncthreads();
  ST(4);
  if (wave < 4) {
    const int h = wave;
    float M = -INFINITY;
#pragma unroll
    for (int w = 0; w < WAVES; ++w) M = fmaxf(M, reinterpret_cast<const float*>(lds + L_T + w * TSZ)[1024 + h]);
    float L = 0.f, o0 = 0.f, o1 = 0.f;
#pragma unroll
    for (int w = 0; w < WAVES; ++w) {
      const float* rw = reinterpret_cast<const float*>(lds + L_T + w * TSZ);
      const float f = __builtin_amdgcn_exp2f(rw[1024 + h] - M);
      const f32x2 hi = *reinterpret_cast<const f32x2*>(rw + h * D + 2 * lane), lo = *reinterpret_cast<const f32x2*>(rw + (4 + h) * D + 2 * lane);
      const float z = rw[1032 + h];
      L = fmaf(f, rw[1028 + h], L);
      o0 = fmaf(f, (hi[0] + lo[0]) - z, o0);
      o1 = fmaf(f, (hi[1] + lo[1]) - z, o1);
    }
    const unsigned rbase = WS_REC + row * NSPLIT * RECSZ;
    __builtin_amdgcn_raw_buffer_store_b128(u32x4{__float_as_uint(o0), tag, __float_as_uint(o1), tag}, rws, lane * 16, rbase + split * RECSZ, 16);
    if (lane == 0) __builtin_amdgcn_raw_buffer_store_b128(u32x4{__float_as_uint(M), tag, __float_as_uint(L), tag}, rws, 1024, rbase + split * RECSZ, 16);
    ST(5);
#pragma clang loop unroll(disable)
    for (int look = 0; look < 2; ++look) {
      u32x4 g[NSPLIT];
      unsigned rb = rbase;
      asm volatile("" : "+s"(rb) :: "memory");
      u32x4 ml = __builtin_amdgcn_raw_buffer_load_b128(rws, (lane & 7) * RECSZ + 1024, rb, 16);
#pragma unroll
      for (int s = 0; s < NSPLIT; ++s) g[s] = __builtin_amdgcn_raw_buffer_load_b128(rws, lane * 16, rb + s * RECSZ, 16);
      asm volatile("s_waitcnt vmcnt(0)" ::: "memory");
      if ((lane & 7) == split) ml = u32x4{__float_as_uint(M), tag, __float_as_uint(L), tag};
      bool ok = ml[1] == tag && ml[3] == tag;
#pragma unroll
      for (int s = 0; s < NSPLIT; ++s) {
        if (s == split) g[s] = u32x4{__float_as_uint(o0), tag, __float_as_uint(o1), tag};
        ok = ok && g[s][1] == tag && g[s][3] == tag;
      }
      if (__all(ok)) {
        float ms[NSPLIT], Mx = -INFINITY;
#pragma unroll
        for (int s = 0; s < NSPLIT; ++s) { ms[s] = __uint_as_float(__builtin_amdgcn_readlane(ml[0], s)); Mx = fmaxf(Mx, ms[s]); }
        float Lx = 0.f, a0 = 0.f, a1 = 0.f;
#pragma unroll
        for (int s = 0; s < NSPLIT; ++s) {
          const float f = __builtin_amdgcn_exp2f(ms[s] - Mx);
          Lx = fmaf(f, __uint_as_float(__builtin_amdgcn_readlane(ml[2], s)), Lx);
          a0 = fmaf(f, __uint_as_float(g[s][0]), a0);
          a1 = fmaf(f, __uint_as_float(g[s][2]), a1);
        }
        out[row * (D / 2) + lane] = pk_bf16(a0 / Lx, a1 / Lx);
        if (lane == 0) __builtin_amdgcn_raw_buffer_store_b32(tag, rws, 0, WS_EP + row * 64, 16);
        break;
      }
    }
    ST(6);
  }
  if (lane == 0) {
    u64s* p = reinterpret_cast<u64s*>(reinterpret_cast<char*>(ws) + STAMP_OFF) + (size_t)(blockIdx.x * WAVES + wave) * NSTAMP;
#pragma unroll
    for (int k = 0; k < NSTAMP; ++k) p[k] = stamp[k];
  }
}

__global__ void __launch_bounds__(1024) probe_k(const unsigned* ws) {
  __shared__ u64s smin[1024], smax[1024], ssum[1024];
  const u64s t0 = rt();
  const u64s* p = reinterpret_cast<const u64s*>(reinterpret_cast<const char*>(ws) + STAMP_OFF);
  const int t = threadIdx.x;
  const u64s a0 = p[t * NSTAMP + 0], ak = p[t * NSTAMP + QK], a3 = p[t * NSTAMP + 3], c7 = p[t * NSTAMP + 7];
  smin[t] = a0; smax[t] = ak; ssum[t] = (QMODE == 2) ? (c7 * 100) / (a3 - a0) : ak;
  __syncthreads();
  for (int o = 512; o; o >>= 1) { if (t < o) { smin[t] = min(smin[t], smin[t + o]); smax[t] = max(smax[t], smax[t + o]); ssum[t] += ssum[t + o]; } __syncthreads(); }
  u64s x;
  if (QMODE == 0) x = smax[0] - smin[0];
  else if (QMODE == 1) x = ssum[0] / 1024 - smin[0];
  else if (QMODE == 2) x = ssum[0] / 1024;
  else x = t0 - smax[0];
  if (x > 20000) x = 20000;
  const u64s until = t0 + 300 + x * AMP;
  while (rt() < until) __builtin_amdgcn_s_sleep(1);
}

extern "C" void kernel_launch(void* const* d_in, const int* in_sizes, int n_in, void* d_out, int out_size,
                              void* d_ws, size_t ws_size, hipStream_t stream) {
  const uint16_t* q = (const uint16_t*)d_in[0];
  const int* qk = (const int*)d_in[1];
  const int* qv = (const int*)d_in[2];
  const uint16_t* ks = (const uint16_t*)d_in[3];
  const uint16_t* kz = (const uint16_t*)d_in[4];
  const uint16_t* vs = (const uint16_t*)d_in[5];
  const uint16_t* vz = (const uint16_t*)d_in[6];
  kv_split<<<NWG, NTHR, 0, stream>>>(q, qk, qv, ks, kz, vs, vz, (unsigned*)d_ws, (unsigned*)d_out);
  probe_k<<<1, 1024, 0, stream>>>((const unsigned*)d_ws);
}
```

```cpp
#include <hip/hip_runtime.h>
#include <hip/hip_bf16.h>
#include <stdint.h>

#define WIN 5
#define NST 16
#define ARGS1 1
#define SLP 0
#define SAUX 16
#define LAUX 16
#define PREWARM 0
#define CSHIFT 8
#define OAUX 0
#define KFIRST 0
#define ROTM 3
#define PAUX 0
#define SPREAD 0

constexpr int B_ = 4, HQ = 32, HKV = 8, SKV = 8192, D = 128, GRP = 64, NG = SKV / GRP, REP = HQ / HKV;
constexpr int NBH = B_ * HKV, NSPLIT = 8, NWG = NBH * NSPLIT, WAVES = 4, NTHR = WAVES * 64;
constexpr unsigned NTILE = SKV / 16;
constexpr unsigned DYN0 = NSPLIT * NST * WAVES;
constexpr unsigned KVBYTES = (unsigned)B_ * HKV * SKV * D * 4u;
constexpr unsigned KPBYTES = (unsigned)B_ * HKV * NG * D * 2u, VPBYTES = (unsigned)B_ * HKV * SKV * 2u;
constexpr int L_KS = 0, L_KZ = 4096, L_VS = 8192, L_VZ = 10240, L_T = 12288;
constexpr int TROW = 272, TSZ = 16 * TROW;
constexpr unsigned WS_EP = 0, WS_CT = 8192, WS_ML = 16384, WS_REC = 16384 + NBH * REP * 1024, WS_END = WS_REC + NBH * REP * 8192;

typedef int i32x4 __attribute__((ext_vector_type(4)));
typedef unsigned u32x4 __attribute__((ext_vector_type(4)));
typedef unsigned u32x2 __attribute__((ext_vector_type(2)));
typedef float f32x4 __attribute__((ext_vector_type(4)));
typedef float f32x2 __attribute__((ext_vector_type(2)));
typedef float f32x16 __attribute__((ext_vector_type(16)));
typedef __bf16 bf16x8 __attribute__((ext_vector_type(8)));
typedef __bf16 bf16x2 __attribute__((ext_vector_type(2)));

__device__ __forceinline__ unsigned pk_bf16(float lo, float hi) {
  f32x2 v = {lo, hi};
  return __builtin_bit_cast(unsigned, __builtin_convertvector(v, bf16x2));
}
__device__ __forceinline__ float rnd_bf16(float x) { return __builtin_bit_cast(float, pk_bf16(0.f, x)); }
__device__ __forceinline__ float bf_lo(unsigned u) { return __builtin_bit_cast(float, u << 16); }
__device__ __forceinline__ float bf_hi(unsigned u) { return __builtin_bit_cast(float, u & 0xffff0000u); }
__device__ __forceinline__ f32x4 mfma16(u32x4 a, u32x4 b, f32x4 c) {
  return __builtin_amdgcn_mfma_f32_16x16x32_bf16(__builtin_bit_cast(bf16x8, a), __builtin_bit_cast(bf16x8, b), c, 0, 0, 0);
}
__device__ __forceinline__ f32x16 mfma32(u32x4 a, u32x4 b, f32x16 c) {
  return __builtin_amdgcn_mfma_f32_32x32x16_bf16(__builtin_bit_cast(bf16x8, a), __builtin_bit_cast(bf16x8, b), c, 0, 0, 0);
}
__device__ __forceinline__ float xg_max(float x) {
  auto s = __builtin_amdgcn_permlane16_swap(__float_as_uint(x), __float_as_uint(x), false, false);
  x = fmaxf(__uint_as_float(s[0]), __uint_as_float(s[1]));
  auto t = __builtin_amdgcn_permlane32_swap(__float_as_uint(x), __float_as_uint(x), false, false);
  return fmaxf(__uint_as_float(t[0]), __uint_as_float(t[1]));
}
__device__ __forceinline__ float xg_sum(float x) {
  auto s = __builtin_amdgcn_permlane16_swap(__float_as_uint(x), __float_as_uint(x), false, false);
  x = __uint_as_float(s[0]) + __uint_as_float(s[1]);
  auto t = __builtin_amdgcn_permlane32_swap(__float_as_uint(x), __float_as_uint(x), false, false);
  return __uint_as_float(t[0]) + __uint_as_float(t[1]);
}
__device__ __forceinline__ float ror8(float x) {
  return __builtin_bit_cast(float, __builtin_amdgcn_mov_dpp(__builtin_bit_cast(int, x), 0x128, 0xf, 0xf, true));
}
__device__ __forceinline__ __amdgpu_buffer_rsrc_t mkrsrc(const void* p, unsigned bytes) {
  return __builtin_amdgcn_make_buffer_rsrc(const_cast<void*>(p), 0, (int)bytes, 0x00020000);
}
#define SB() __builtin_amdgcn_sched_barrier(0)
#define ST(k)

__global__ void __launch_bounds__(NTHR, 2)
kv_decode(const uint16_t* __restrict__ q, const int* __restrict__ qk, const int* __restrict__ qv,
          const uint16_t* __restrict__ ks, const uint16_t* __restrict__ kz,
          const uint16_t* __restrict__ vs, const uint16_t* __restrict__ vz,
          unsigned* ws, unsigned* out) {
  __shared__ __attribute__((aligned(16))) char lds_all[L_T + WAVES * TSZ];
  char* const lds = lds_all + L_T;
  ST(0);
  asm volatile("" :: "s"(q), "s"(qk), "s"(qv), "s"(ks), "s"(kz), "s"(vs), "s"(vz), "s"(ws), "s"(out));
  const unsigned bid = SPREAD ? blockIdx.x : (blockIdx.x & 7) * (NWG / 8) + (blockIdx.x >> 3);
  const unsigned bh = bid / NSPLIT, split = bid % NSPLIT;
  const int tid = threadIdx.x, lane = tid & 63;
  const unsigned wave = __builtin_amdgcn_readfirstlane(tid >> 6);
  const int c = lane & 15, g4 = lane >> 4, hh = lane >> 5, ch = lane & 31;
  unsigned rnd = (ROTM == 1 ? blockIdx.x >> 3 : ROTM == 3 ? blockIdx.x >> 4 : ROTM == 4 ? (blockIdx.x >> 3) * 2 : 0) & (NST - 1);

  const unsigned vo0 = lane * 16, vo1 = lane * 16 + 4096;
  u32x4 buf[16];
  auto issue = [&](int slot, unsigned tile) __attribute__((always_inline)) {
    const int n = slot & 7;
    buf[slot] = __builtin_amdgcn_raw_buffer_load_b128(mkrsrc(slot < 8 ? qk : qv, tile < NTILE ? KVBYTES : 0u),
                                                      (n < 4 ? vo0 : vo1) + (n & 3) * 1024, (bh * NTILE + (tile & (NTILE - 1))) * 8192u, 2);
  };
  unsigned cur = (split * NST + rnd) * WAVES + wave;
  const __amdgpu_buffer_rsrc_t rws = mkrsrc(ws, WS_END);
  const u32x4 st0 = __builtin_amdgcn_raw_buffer_load_b128(mkrsrc(ks, KPBYTES), tid * 16, bid * 4096, PAUX);
  const u32x4 st1 = __builtin_amdgcn_raw_buffer_load_b128(mkrsrc(kz, KPBYTES), tid * 16, bid * 4096, PAUX);
  const u32x4 st2 = __builtin_amdgcn_raw_buffer_load_b128(mkrsrc(wave < 2 ? vs : vz, VPBYTES), (tid & 127) * 16, bid * 2048, PAUX);
  const unsigned row = bh * REP + wave;
  const unsigned rowc = ((bh + CSHIFT) & (NBH - 1)) * REP + wave;
  const unsigned epp = __builtin_amdgcn_raw_buffer_load_b32(rws, (lane >> 3) * 4, WS_EP + row * 32, 16);
  const unsigned epc = __builtin_amdgcn_raw_buffer_load_b32(rws, 0, WS_EP + rowc * 32 + split * 4, 16);
  u32x4 Bq[4];
#pragma unroll
  for (int m = 0; m < 4; ++m)
    Bq[m] = __builtin_amdgcn_raw_buffer_load_b128(mkrsrc(q, B_ * HQ * D * 2), (c & 3) * 256 + g4 * 16 + m * 64, bh * 1024, 0);
  SB();
#pragma unroll
  for (int i = 0; i < WIN; ++i) issue(i, cur);
  SB();
  ST(2);
  *reinterpret_cast<u32x4*>(lds_all + L_KS + tid * 16) = st0;
  *reinterpret_cast<u32x4*>(lds_all + L_KZ + tid * 16) = st1;
  *reinterpret_cast<u32x4*>(lds_all + L_VS + tid * 16) = st2;
  const unsigned tagp = epp + 0x9E3779B9u, tag = __builtin_amdgcn_readfirstlane(epc) + 0x9E3779B9u;
  __syncthreads();

  char* const tw = lds + wave * TSZ + hh * 8 * TROW + ch * 8;
  const char* const tr = lds + wave * TSZ + c * TROW + g4 * 16;
  const bool lo_lane = (c >> 2) & 1;
  const unsigned sel = hh ? 0x03020c0cu : 0x01000c0cu;
  const unsigned tko = lane == 0 ? 0u : 0x80000000u;
  constexpr float SCALE = 0.088388347648318440f, LOG2E = 1.4426950408889634f;

  float m_run = -INFINITY, l_run = 0.f, z_run = 0.f;
  f32x16 acc[4];
#pragma unroll
  for (int cc = 0; cc < 4; ++cc)
#pragma unroll
    for (int r = 0; r < 16; ++r) acc[cc][r] = 0.f;

#pragma clang loop unroll(disable)
  for (unsigned it = 0; it < NST; ++it) {
    const unsigned lt = cur & 63;
    const u32x2 sraw = *reinterpret_cast<const u32x2*>(lds_all + L_KS + (lt >> 2) * 256 + ch * 8);
    const u32x2 zraw = *reinterpret_cast<const u32x2*>(lds_all + L_KZ + (lt >> 2) * 256 + ch * 8);
    const u32x4 vsr = *reinterpret_cast<const u32x4*>(lds_all + L_VS + lt * 32 + (g4 & 1) * 16);
    const u32x4 vzr = *reinterpret_cast<const u32x4*>(lds_all + L_VZ + lt * 32 + (g4 & 1) * 16);
    const f32x2 s01 = {bf_lo(sraw[0]), bf_hi(sraw[0])}, s23 = {bf_lo(sraw[1]), bf_hi(sraw[1])};
    const f32x2 z01 = {bf_lo(zraw[0]), bf_hi(zraw[0])}, z23 = {bf_lo(zraw[1]), bf_hi(zraw[1])};
    rnd = (rnd + 1) & (NST - 1);
    const unsigned nxt = it + 1 < NST ? (split * NST + rnd) * WAVES + wave : NTILE;
    SB();
#pragma unroll
    for (int n = 0; n < 8; ++n) {
      const i32x4 a = __builtin_bit_cast(i32x4, buf[n]);
      f32x2 t01 = {(float)a[0], (float)a[1]}, t23 = {(float)a[2], (float)a[3]};
      SB();
      { const int j = n + WIN; if (j < 16) issue(j, cur); else issue(j - 16, nxt); }
      SB();
      t01 -= z01; t23 -= z23;
      t01 = f32x2{rnd_bf16(t01[0]), rnd_bf16(t01[1])} * s01;
      t23 = f32x2{rnd_bf16(t23[0]), rnd_bf16(t23[1])} * s23;
      *reinterpret_cast<u32x2*>(tw + n * TROW) = u32x2{pk_bf16(t01[0], t01[1]), pk_bf16(t23[0], t23[1])};
    }
    u32x4 Bv[4];
    auto vpair = [&](int u) __attribute__((always_inline)) {
      SB();
      const i32x4 a = __builtin_bit_cast(i32x4, buf[8 + 2 * u]);
      f32x4 fa = {(float)a[0], (float)a[1], (float)a[2], (float)a[3]};
      SB();
      { const int j = 8 + 2 * u + WIN; if (j < 16) issue(j, cur); else issue(j - 16, nxt); }
      SB();
      const i32x4 b = __builtin_bit_cast(i32x4, buf[9 + 2 * u]);
#pragma unroll
      for (int cc = 0; cc < 4; ++cc) Bv[cc][u] = pk_bf16(fa[cc], (float)b[cc]);
      SB();
      { const int j = 9 + 2 * u + WIN; if (j < 16) issue(j, cur); else issue(j - 16, nxt); }
      SB();
    };
    asm volatile("" ::: "memory");
    f32x4 S = {0.f, 0.f, 0.f, 0.f};
#pragma unroll
    for (int m = 0; m < 4; ++m) S = mfma16(*reinterpret_cast<const u32x4*>(tr + m * 64), Bq[m], S);
    asm volatile("" ::: "memory");
    vpair(0);
    float y[4];
#pragma unroll
    for (int r = 0; r < 4; ++r) y[r] = rnd_bf16(rnd_bf16(S[r]) * SCALE) * LOG2E;
    const float m_new = fmaxf(m_run, xg_max(fmaxf(fmaxf(y[0], y[1]), fmaxf(y[2], y[3]))));
    const float alpha = __builtin_amdgcn_exp2f(m_run - m_new);
    m_run = m_new;
    float p[4];
#pragma unroll
    for (int r = 0; r < 4; ++r) p[r] = __builtin_amdgcn_exp2f(y[r] - m_new);
    vpair(1);
    float pv[4];
    float zs = 0.f;
#pragma unroll
    for (int r = 0; r < 4; ++r) {
      pv[r] = p[r] * __builtin_bit_cast(float, __builtin_amdgcn_perm(vsr[r], vsr[r], sel));
      zs = fmaf(pv[r], __builtin_bit_cast(float, __builtin_amdgcn_perm(vzr[r], vzr[r], sel)), zs);
    }
    l_run = fmaf(l_run, alpha, (p[0] + p[1]) + (p[2] + p[3]));
    z_run = fmaf(z_run, alpha, zs);
    unsigned X[2];
#pragma unroll
    for (int u = 0; u < 2; ++u) {
      const unsigned h = pk_bf16(pv[2 * u], pv[2 * u + 1]);
      const unsigned l = pk_bf16(pv[2 * u] - bf_lo(h), pv[2 * u + 1] - bf_hi(h));
      X[u] = lo_lane ? l : h;
    }
    vpair(2);
    const auto w0 = __builtin_amdgcn_permlane16_swap(X[0], X[0], false, false);
    const auto w1 = __builtin_amdgcn_permlane16_swap(X[1], X[1], false, false);
    const u32x4 P = {w0[0], w1[0], w0[1], w1[1]};
#pragma unroll
    for (int r = 0; r < 4; ++r) {
      const float a = __builtin_bit_cast(float, __builtin_amdgcn_readlane(__builtin_bit_cast(int, alpha), r));
#pragma unroll
      for (int cc = 0; cc < 4; ++cc) acc[cc][r] *= a;
    }
    vpair(3);
#pragma unroll
    for (int cc = 0; cc < 4; ++cc) acc[cc] = mfma32(P, Bv[cc], acc[cc]);
    cur = nxt;
  }
  ST(3);

  u32x4 g = __builtin_amdgcn_raw_buffer_load_b128(rws, lane * 16, WS_REC + rowc * 8192 + split * 1024, 16);
  u32x4 ml = __builtin_amdgcn_raw_buffer_load_b128(rws, (lane >> 3) * 16, WS_ML + rowc * 1024 + split * 128, 16);
  SB();
  l_run = xg_sum(l_run);
  z_run = xg_sum(z_run);
  float* rec = reinterpret_cast<float*>(lds + wave * TSZ);
#pragma unroll
  for (int r = 0; r < 4; ++r)
    *reinterpret_cast<f32x4*>(rec + (hh * 4 + r) * D + 4 * ch) = f32x4{acc[0][r], acc[1][r], acc[2][r], acc[3][r]};
  if (lane < 4) *reinterpret_cast<f32x4*>(rec + 1024 + 4 * lane) = f32x4{m_run, l_run, z_run, 0.f};
  __syncthreads();
  ST(4);
  f32x4 mlz[WAVES];
  f32x2 hi[WAVES], lo[WAVES];
#pragma unroll
  for (int w = 0; w < WAVES; ++w) {
    const float* rw = reinterpret_cast<const float*>(lds + w * TSZ);
    mlz[w] = *reinterpret_cast<const f32x4*>(rw + 1024 + 4 * wave);
    hi[w] = *reinterpret_cast<const f32x2*>(rw + wave * D + 2 * lane);
    lo[w] = *reinterpret_cast<const f32x2*>(rw + (4 + wave) * D + 2 * lane);
  }
  SB();
  const float M = fmaxf(fmaxf(mlz[0][0], mlz[1][0]), fmaxf(mlz[2][0], mlz[3][0]));
  float L = 0.f, o0 = 0.f, o1 = 0.f;
#pragma unroll
  for (int w = 0; w < WAVES; ++w) {
    const float f = __builtin_amdgcn_exp2f(mlz[w][0] - M);
    L = fmaf(f, mlz[w][1], L);
    o0 = fmaf(f, (hi[w][0] + lo[w][0]) - mlz[w][2], o0);
    o1 = fmaf(f, (hi[w][1] + lo[w][1]) - mlz[w][2], o1);
  }
  __builtin_amdgcn_raw_buffer_store_b128(u32x4{__float_as_uint(o0), tagp, __float_as_uint(o1), tagp}, rws,
                                         (lane >> 3) * 1024 + (lane & 7) * 16, WS_REC + row * 8192 + split * 128, 16);
  __builtin_amdgcn_raw_buffer_store_b128(u32x4{__float_as_uint(M), tagp, __float_as_uint(L), tagp}, rws,
                                         (lane & 7) == 0 ? lane * 16u : 0x80000000u, WS_ML + row * 1024 + split * 16, 16);
  ST(5);
  bool need = !(g[1] == tag && g[3] == tag && ml[1] == tag && ml[3] == tag);
#pragma clang loop unroll(disable)
  for (unsigned spin = 0; spin < (1u << 20); ++spin) {
    if (!__any(need)) break;
    if (need) {
      unsigned rb = rowc;
      asm volatile("" : "+s"(rb) :: "memory");
      g = __builtin_amdgcn_raw_buffer_load_b128(rws, lane * 16, WS_REC + rb * 8192 + split * 1024, 16);
      ml = __builtin_amdgcn_raw_buffer_load_b128(rws, (lane >> 3) * 16, WS_ML + rb * 1024 + split * 128, 16);
      need = !(g[1] == tag && g[3] == tag && ml[1] == tag && ml[3] == tag);
    }
    if (SLP) __builtin_amdgcn_s_sleep(SLP);
  }
  ST(6);
  const float mp = __uint_as_float(ml[0]);
  const float Mx = xg_max(fmaxf(mp, ror8(mp)));
  const float f = __builtin_amdgcn_exp2f(mp - Mx);
  float a0 = f * __uint_as_float(g[0]), a1 = f * __uint_as_float(g[2]), Lx = f * __uint_as_float(ml[2]);
  a0 = xg_sum(a0 + ror8(a0));
  a1 = xg_sum(a1 + ror8(a1));
  Lx = xg_sum(Lx + ror8(Lx));
  __builtin_amdgcn_raw_buffer_store_b32(pk_bf16(a0 * __builtin_amdgcn_rcpf(Lx), a1 * __builtin_amdgcn_rcpf(Lx)), mkrsrc(out, B_ * HQ * D * 2), lane < 8 ? lane * 4u : 0x80000000u, rowc * 256 + split * 32, OAUX);
  __builtin_amdgcn_raw_buffer_store_b32(tag, rws, tko, WS_EP + rowc * 32 + split * 4, OAUX);
  ST(7);
}

extern "C" void kernel_launch(void* const* d_in, const int* in_sizes, int n_in, void* d_out, int out_size,
                              void* d_ws, size_t ws_size, hipStream_t stream) {
  kv_decode<<<NWG, NTHR, 0, stream>>>((const uint16_t*)d_in[0], (const int*)d_in[1], (const int*)d_in[2],
                                      (const uint16_t*)d_in[3], (const uint16_t*)d_in[4],
                                      (const uint16_t*)d_in[5], (const uint16_t*)d_in[6],
                                      (unsigned*)d_ws, (unsigned*)d_out);
}
```

```cpp
#include <hip/hip_runtime.h>
#include <hip/hip_bf16.h>
#include <stdint.h>

#define WIN 8
#define NST 16
#define ARGS1 1
#define SLP 0
#define SAUX 16
#define LAUX 16
#define PREWARM 0
#define CSHIFT 8
#define OAUX 0
#define WLO 5
#define TSCH 3560
#define TOFF 100
#define NCHK 4
#define CODEPF 0
#define CODEBYTES 6144
#define EAUX 16
#define MLPRIV 0
#define KFIRST 0
#define ROTM 3
#define PAUX 0
#define SPREAD 0

constexpr int B_ = 4, HQ = 32, HKV = 8, SKV = 8192, D = 128, GRP = 64, NG = SKV / GRP, REP = HQ / HKV;
constexpr int NBH = B_ * HKV, NSPLIT = 8, NWG = NBH * NSPLIT, WAVES = 4, NTHR = WAVES * 64;
constexpr unsigned NTILE = SKV / 16;
constexpr unsigned DYN0 = NSPLIT * NST * WAVES;
constexpr unsigned KVBYTES = (unsigned)B_ * HKV * SKV * D * 4u;
constexpr unsigned KPBYTES = (unsigned)B_ * HKV * NG * D * 2u, VPBYTES = (unsigned)B_ * HKV * SKV * 2u;
constexpr int L_KS = 0, L_KZ = 4096, L_VS = 8192, L_VZ = 10240, L_T = 12288;
constexpr int TROW = 272, TSZ = 16 * TROW;
constexpr unsigned WS_EP = 0, WS_CT = 8192, MLS = MLPRIV ? 8 : 1, WS_ML = 16384, WS_REC = 16384 + NBH * REP * 1024 * MLS, WS_END = WS_REC + NBH * REP * 8192;

typedef int i32x4 __attribute__((ext_vector_type(4)));
typedef unsigned u32x4 __attribute__((ext_vector_type(4)));
typedef unsigned u32x2 __attribute__((ext_vector_type(2)));
typedef float f32x4 __attribute__((ext_vector_type(4)));
typedef float f32x2 __attribute__((ext_vector_type(2)));
typedef float f32x16 __attribute__((ext_vector_type(16)));
typedef __bf16 bf16x8 __attribute__((ext_vector_type(8)));
typedef __bf16 bf16x2 __attribute__((ext_vector_type(2)));

__device__ __forceinline__ unsigned pk_bf16(float lo, float hi) {
  f32x2 v = {lo, hi};
  return __builtin_bit_cast(unsigned, __builtin_convertvector(v, bf16x2));
}
__device__ __forceinline__ float rnd_bf16(float x) { return __builtin_bit_cast(float, pk_bf16(0.f, x)); }
__device__ __forceinline__ float bf_lo(unsigned u) { return __builtin_bit_cast(float, u << 16); }
__device__ __forceinline__ float bf_hi(unsigned u) { return __builtin_bit_cast(float, u & 0xffff0000u); }
__device__ __forceinline__ f32x4 mfma16(u32x4 a, u32x4 b, f32x4 c) {
  return __builtin_amdgcn_mfma_f32_16x16x32_bf16(__builtin_bit_cast(bf16x8, a), __builtin_bit_cast(bf16x8, b), c, 0, 0, 0);
}
__device__ __forceinline__ f32x16 mfma32(u32x4 a, u32x4 b, f32x16 c) {
  return __builtin_amdgcn_mfma_f32_32x32x16_bf16(__builtin_bit_cast(bf16x8, a), __builtin_bit_cast(bf16x8, b), c, 0, 0, 0);
}
__device__ __forceinline__ float xg_max(float x) {
  auto s = __builtin_amdgcn_permlane16_swap(__float_as_uint(x), __float_as_uint(x), false, false);
  x = fmaxf(__uint_as_float(s[0]), __uint_as_float(s[1]));
  auto t = __builtin_amdgcn_permlane32_swap(__float_as_uint(x), __float_as_uint(x), false, false);
  return fmaxf(__uint_as_float(t[0]), __uint_as_float(t[1]));
}
__device__ __forceinline__ float xg_sum(float x) {
  auto s = __builtin_amdgcn_permlane16_swap(__float_as_uint(x), __float_as_uint(x), false, false);
  x = __uint_as_float(s[0]) + __uint_as_float(s[1]);
  auto t = __builtin_amdgcn_permlane32_swap(__float_as_uint(x), __float_as_uint(x), false, false);
  return __uint_as_float(t[0]) + __uint_as_float(t[1]);
}
__device__ __forceinline__ float ror8(float x) {
  return __builtin_bit_cast(float, __builtin_amdgcn_mov_dpp(__builtin_bit_cast(int, x), 0x128, 0xf, 0xf, true));
}
__device__ __forceinline__ __amdgpu_buffer_rsrc_t mkrsrc(const void* p, unsigned bytes) {
  return __builtin_amdgcn_make_buffer_rsrc(const_cast<void*>(p), 0, (int)bytes, 0x00020000);
}
#define SB() __builtin_amdgcn_sched_barrier(0)
__device__ __forceinline__ unsigned now32() {
  unsigned long long t; asm volatile("s_memrealtime %0\n\ts_waitcnt lgkmcnt(0)" : "=s"(t) :: "memory"); return (unsigned)t;
}
#define ST(k)

__global__ void __launch_bounds__(NTHR, 2)
kv_decode(const uint16_t* __restrict__ q, const int* __restrict__ qk, const int* __restrict__ qv,
          const uint16_t* __restrict__ ks, const uint16_t* __restrict__ kz,
          const uint16_t* __restrict__ vs, const uint16_t* __restrict__ vz,
          unsigned* ws, unsigned* out) {
  __shared__ __attribute__((aligned(16))) char lds_all[L_T + WAVES * TSZ];
  char* const lds = lds_all + L_T;
  ST(0);
  asm volatile("" :: "s"(q), "s"(qk), "s"(qv), "s"(ks), "s"(kz), "s"(vs), "s"(vz), "s"(ws), "s"(out));
  const unsigned tstart = now32();
  const unsigned bid = SPREAD ? blockIdx.x : (blockIdx.x & 7) * (NWG / 8) + (blockIdx.x >> 3);
  const unsigned bh = bid / NSPLIT, split = bid % NSPLIT;
  const int tid = threadIdx.x, lane = tid & 63;
  const unsigned wave = __builtin_amdgcn_readfirstlane(tid >> 6);
  const int c = lane & 15, g4 = lane >> 4, hh = lane >> 5, ch = lane & 31;
  unsigned rnd = (ROTM == 1 ? blockIdx.x >> 3 : ROTM == 3 ? blockIdx.x >> 4 : ROTM == 4 ? (blockIdx.x >> 3) * 2 : 0) & (NST - 1);

  const unsigned vo0 = lane * 16, vo1 = lane * 16 + 4096;
  u32x4 buf[16];
  auto issue = [&](int slot, unsigned tile) __attribute__((always_inline)) {
    const int n = slot & 7;
    buf[slot] = __builtin_amdgcn_raw_buffer_load_b128(mkrsrc(slot < 8 ? qk : qv, tile < NTILE ? KVBYTES : 0u),
                                                      (n < 4 ? vo0 : vo1) + (n & 3) * 1024, (bh * NTILE + (tile & (NTILE - 1))) * 8192u, 2);
  };
  unsigned cur = (split * NST + rnd) * WAVES + wave;
  const __amdgpu_buffer_rsrc_t rws = mkrsrc(ws, WS_END);
  const u32x4 st0 = __builtin_amdgcn_raw_buffer_load_b128(mkrsrc(ks, KPBYTES), tid * 16, bid * 4096, PAUX);
  const u32x4 st1 = __builtin_amdgcn_raw_buffer_load_b128(mkrsrc(kz, KPBYTES), tid * 16, bid * 4096, PAUX);
  const u32x4 st2 = __builtin_amdgcn_raw_buffer_load_b128(mkrsrc(wave < 2 ? vs : vz, VPBYTES), (tid & 127) * 16, bid * 2048, PAUX);
  const unsigned row = bh * REP + wave;
  const unsigned rowc = ((bh + CSHIFT) & (NBH - 1)) * REP + wave;
  const unsigned epp = __builtin_amdgcn_raw_buffer_load_b32(rws, (lane >> 3) * 4, WS_EP + row * 32, EAUX);
  const unsigned epc = __builtin_amdgcn_raw_buffer_load_b32(rws, 0, WS_EP + rowc * 32 + split * 4, EAUX);
  u32x4 Bq[4];
#pragma unroll
  for (int m = 0; m < 4; ++m)
    Bq[m] = __builtin_amdgcn_raw_buffer_load_b128(mkrsrc(q, B_ * HQ * D * 2), (c & 3) * 256 + g4 * 16 + m * 64, bh * 1024, 0);
  SB();
#pragma unroll
  for (int i = 0; i < WIN; ++i) issue(i, cur);
  SB();
  ST(2);
  *reinterpret_cast<u32x4*>(lds_all + L_KS + tid * 16) = st0;
  *reinterpret_cast<u32x4*>(lds_all + L_KZ + tid * 16) = st1;
  *reinterpret_cast<u32x4*>(lds_all + L_VS + tid * 16) = st2;
  const unsigned tagp = epp + 0x9E3779B9u, tag = __builtin_amdgcn_readfirstlane(epc) + 0x9E3779B9u;
  __syncthreads();

  char* const tw = lds + wave * TSZ + hh * 8 * TROW + ch * 8;
  const char* const tr = lds + wave * TSZ + c * TROW + g4 * 16;
  const bool lo_lane = (c >> 2) & 1;
  const unsigned sel = hh ? 0x03020c0cu : 0x01000c0cu;
  const unsigned tko = lane == 0 ? 0u : 0x80000000u;
  constexpr float SCALE = 0.088388347648318440f, LOG2E = 1.4426950408889634f;

  unsigned ahead = 0;
  auto check = [&](int idx) __attribute__((always_inline)) {
    ahead = (int)((now32() - tstart - TOFF) * 256u - (unsigned)(idx - WLO) * TSCH) < 0;
  };
  auto throttle = [&]() __attribute__((always_inline)) {
    if (WLO < WIN) asm volatile("s_cmp_eq_u32 %0, 0\n\ts_cbranch_scc1 1f\n\ts_waitcnt vmcnt(%1)\n1:" :: "s"(ahead), "n"(WLO - 1) : "scc", "memory");
  };
  float m_run = -INFINITY, l_run = 0.f, z_run = 0.f;
  f32x16 acc[4];
#pragma unroll
  for (int cc = 0; cc < 4; ++cc)
#pragma unroll
    for (int r = 0; r < 16; ++r) acc[cc][r] = 0.f;

#pragma clang loop unroll(disable)
  for (unsigned it = 0; it < NST; ++it) {
    const unsigned lt = cur & 63;
    const u32x2 sraw = *reinterpret_cast<const u32x2*>(lds_all + L_KS + (lt >> 2) * 256 + ch * 8);
    const u32x2 zraw = *reinterpret_cast<const u32x2*>(lds_all + L_KZ + (lt >> 2) * 256 + ch * 8);
    const u32x4 vsr = *reinterpret_cast<const u32x4*>(lds_all + L_VS + lt * 32 + (g4 & 1) * 16);
    const u32x4 vzr = *reinterpret_cast<const u32x4*>(lds_all + L_VZ + lt * 32 + (g4 & 1) * 16);
    const f32x2 s01 = {bf_lo(sraw[0]), bf_hi(sraw[0])}, s23 = {bf_lo(sraw[1]), bf_hi(sraw[1])};
    const f32x2 z01 = {bf_lo(zraw[0]), bf_hi(zraw[0])}, z23 = {bf_lo(zraw[1]), bf_hi(zraw[1])};
    rnd = (rnd + 1) & (NST - 1);
    const unsigned nxt = it + 1 < NST ? (split * NST + rnd) * WAVES + wave : NTILE;
    SB();
#pragma unroll
    for (int n = 0; n < 8; ++n) {
      const i32x4 a = __builtin_bit_cast(i32x4, buf[n]);
      f32x2 t01 = {(float)a[0], (float)a[1]}, t23 = {(float)a[2], (float)a[3]};
      SB();
      { const int j = n + WIN; if (n == 0 || (NCHK == 4 && n == 4)) check(it * 16 + j); throttle(); if (j < 16) issue(j, cur); else issue(j - 16, nxt); }
      SB();
      t01 -= z01; t23 -= z23;
      t01 = f32x2{rnd_bf16(t01[0]), rnd_bf16(t01[1])} * s01;
      t23 = f32x2{rnd_bf16(t23[0]), rnd_bf16(t23[1])} * s23;
      *reinterpret_cast<u32x2*>(tw + n * TROW) = u32x2{pk_bf16(t01[0], t01[1]), pk_bf16(t23[0], t23[1])};
    }
    u32x4 Bv[4];
    auto vpair = [&](int u) __attribute__((always_inline)) {
      SB();
      const i32x4 a = __builtin_bit_cast(i32x4, buf[8 + 2 * u]);
      f32x4 fa = {(float)a[0], (float)a[1], (float)a[2], (float)a[3]};
      asm volatile("" : "+v"(fa));
      SB();
      { const int j = 8 + 2 * u + WIN; if (u == 0 || (NCHK == 4 && u == 2)) check(it * 16 + j); throttle(); if (j < 16) issue(j, cur); else issue(j - 16, nxt); }
      SB();
      const i32x4 b = __builtin_bit_cast(i32x4, buf[9 + 2 * u]);
#pragma unroll
      for (int cc = 0; cc < 4; ++cc) Bv[cc][u] = pk_bf16(fa[cc], (float)b[cc]);
      SB();
      { const int j = 9 + 2 * u + WIN; throttle(); if (j < 16) issue(j, cur); else issue(j - 16, nxt); }
      SB();
    };
    asm volatile("" ::: "memory");
    f32x4 S = {0.f, 0.f, 0.f, 0.f};
#pragma unroll
    for (int m = 0; m < 4; ++m) S = mfma16(*reinterpret_cast<const u32x4*>(tr + m * 64), Bq[m], S);
    asm volatile("" ::: "memory");
    vpair(0);
    float y[4];
#pragma unroll
    for (int r = 0; r < 4; ++r) y[r] = rnd_bf16(rnd_bf16(S[r]) * SCALE) * LOG2E;
    const float m_new = fmaxf(m_run, xg_max(fmaxf(fmaxf(y[0], y[1]), fmaxf(y[2], y[3]))));
    const float alpha = __builtin_amdgcn_exp2f(m_run - m_new);
    m_run = m_new;
    float p[4];
#pragma unroll
    for (int r = 0; r < 4; ++r) p[r] = __builtin_amdgcn_exp2f(y[r] - m_new);
    vpair(1);
    float pv[4];
    float zs = 0.f;
#pragma unroll
    for (int r = 0; r < 4; ++r) {
      pv[r] = p[r] * __builtin_bit_cast(float, __builtin_amdgcn_perm(vsr[r], vsr[r], sel));
      zs = fmaf(pv[r], __builtin_bit_cast(float, __builtin_amdgcn_perm(vzr[r], vzr[r], sel)), zs);
    }
    l_run = fmaf(l_run, alpha, (p[0] + p[1]) + (p[2] + p[3]));
    z_run = fmaf(z_run, alpha, zs);
    unsigned X[2];
#pragma unroll
    for (int u = 0; u < 2; ++u) {
      const unsigned h = pk_bf16(pv[2 * u], pv[2 * u + 1]);
      const unsigned l = pk_bf16(pv[2 * u] - bf_lo(h), pv[2 * u + 1] - bf_hi(h));
      X[u] = lo_lane ? l : h;
    }
    vpair(2);
    const auto w0 = __builtin_amdgcn_permlane16_swap(X[0], X[0], false, false);
    const auto w1 = __builtin_amdgcn_permlane16_swap(X[1], X[1], false, false);
    const u32x4 P = {w0[0], w1[0], w0[1], w1[1]};
#pragma unroll
    for (int r = 0; r < 4; ++r) {
      const float a = __builtin_bit_cast(float, __builtin_amdgcn_readlane(__builtin_bit_cast(int, alpha), r));
#pragma unroll
      for (int cc = 0; cc < 4; ++cc) acc[cc][r] *= a;
    }
    vpair(3);
#pragma unroll
    for (int cc = 0; cc < 4; ++cc) acc[cc] = mfma32(P, Bv[cc], acc[cc]);
    cur = nxt;
  }
  ST(3);

  u32x4 g = __builtin_amdgcn_raw_buffer_load_b128(rws, lane * 16, WS_REC + rowc * 8192 + split * 1024, 16);
  u32x4 ml = __builtin_amdgcn_raw_buffer_load_b128(rws, (lane >> 3) * 16 * MLS, WS_ML + (rowc * 1024 + split * 128) * MLS, 16);
  SB();
  l_run = xg_sum(l_run);
  z_run = xg_sum(z_run);
  float* rec = reinterpret_cast<float*>(lds + wave * TSZ);
#pragma unroll
  for (int r = 0; r < 4; ++r)
    *reinterpret_cast<f32x4*>(rec + (hh * 4 + r) * D + 4 * ch) = f32x4{acc[0][r], acc[1][r], acc[2][r], acc[3][r]};
  if (lane < 4) *reinterpret_cast<f32x4*>(rec + 1024 + 4 * lane) = f32x4{m_run, l_run, z_run, 0.f};
  __syncthreads();
  ST(4);
  f32x4 mlz[WAVES];
  f32x2 hi[WAVES], lo[WAVES];
#pragma unroll
  for (int w = 0; w < WAVES; ++w) {
    const float* rw = reinterpret_cast<const float*>(lds + w * TSZ);
    mlz[w] = *reinterpret_cast<const f32x4*>(rw + 1024 + 4 * wave);
    hi[w] = *reinterpret_cast<const f32x2*>(rw + wave * D + 2 * lane);
    lo[w] = *reinterpret_cast<const f32x2*>(rw + (4 + wave) * D + 2 * lane);
  }
  SB();
  const float M = fmaxf(fmaxf(mlz[0][0], mlz[1][0]), fmaxf(mlz[2][0], mlz[3][0]));
  float L = 0.f, o0 = 0.f, o1 = 0.f;
#pragma unroll
  for (int w = 0; w < WAVES; ++w) {
    const float f = __builtin_amdgcn_exp2f(mlz[w][0] - M);
    L = fmaf(f, mlz[w][1], L);
    o0 = fmaf(f, (hi[w][0] + lo[w][0]) - mlz[w][2], o0);
    o1 = fmaf(f, (hi[w][1] + lo[w][1]) - mlz[w][2], o1);
  }
  __builtin_amdgcn_raw_buffer_store_b128(u32x4{__float_as_uint(o0), tagp, __float_as_uint(o1), tagp}, rws,
                                         (lane >> 3) * 1024 + (lane & 7) * 16, WS_REC + row * 8192 + split * 128, 16);
  __builtin_amdgcn_raw_buffer_store_b128(u32x4{__float_as_uint(M), tagp, __float_as_uint(L), tagp}, rws,
                                         (lane & 7) == 0 ? lane * 16u * MLS : 0x80000000u, WS_ML + (row * 1024 + split * 16) * MLS, 16);
  ST(5);
  bool need = !(g[1] == tag && g[3] == tag && ml[1] == tag && ml[3] == tag);
#pragma clang loop unroll(disable)
  for (unsigned spin = 0; spin < (1u << 20); ++spin) {
    if (!__any(need)) break;
    if (need) {
      unsigned rb = rowc;
      asm volatile("" : "+s"(rb) :: "memory");
      g = __builtin_amdgcn_raw_buffer_load_b128(rws, lane * 16, WS_REC + rb * 8192 + split * 1024, 16);
      ml = __builtin_amdgcn_raw_buffer_load_b128(rws, (lane >> 3) * 16 * MLS, WS_ML + (rb * 1024 + split * 128) * MLS, 16);
      need = !(g[1] == tag && g[3] == tag && ml[1] == tag && ml[3] == tag);
    }
    if (SLP) __builtin_amdgcn_s_sleep(SLP);
  }
  ST(6);
  const float mp = __uint_as_float(ml[0]);
  const float Mx = xg_max(fmaxf(mp, ror8(mp)));
  const float f = __builtin_amdgcn_exp2f(mp - Mx);
  float a0 = f * __uint_as_float(g[0]), a1 = f * __uint_as_float(g[2]), Lx = f * __uint_as_float(ml[2]);
  a0 = xg_sum(a0 + ror8(a0));
  a1 = xg_sum(a1 + ror8(a1));
  Lx = xg_sum(Lx + ror8(Lx));
  __builtin_amdgcn_raw_buffer_store_b32(pk_bf16(a0 * __builtin_amdgcn_rcpf(Lx), a1 * __builtin_amdgcn_rcpf(Lx)), mkrsrc(out, B_ * HQ * D * 2), lane < 8 ? lane * 4u : 0x80000000u, rowc * 256 + split * 32, OAUX);
  __builtin_amdgcn_raw_buffer_store_b32(tag, rws, tko, WS_EP + rowc * 32 + split * 4, OAUX);
  ST(7);
}

extern "C" void kernel_launch(void* const* d_in, const int* in_sizes, int n_in, void* d_out, int out_size,
                              void* d_ws, size_t ws_size, hipStream_t stream) {
  kv_decode<<<NWG, NTHR, 0, stream>>>((const uint16_t*)d_in[0], (const int*)d_in[1], (const int*)d_in[2],
                                      (const uint16_t*)d_in[3], (const uint16_t*)d_in[4],
                                      (const uint16_t*)d_in[5], (const uint16_t*)d_in[6],
                                      (unsigned*)d_ws, (unsigned*)d_out);
}
```

```cpp
#include <hip/hip_runtime.h>
#include <hip/hip_bf16.h>
#include <stdint.h>

#define WIN 8
#define WLO 5
#define WHI1 8
#define WLO2 4
#define WHI2 7
#define TOUCH 0
#define TSCH 3450
#define TOFF 140
#define PAT 3
#define CSHIFT 8

constexpr int B_ = 4, HQ = 32, HKV = 8, SKV = 8192, D = 128, GRP = 64, NG = SKV / GRP, REP = HQ / HKV;
constexpr int NBH = B_ * HKV, NSPLIT = 8, NWG = NBH * NSPLIT, WAVES = 4, NTHR = WAVES * 64, NST = 16;
constexpr unsigned KVBYTES = (unsigned)B_ * HKV * SKV * D * 4u;
constexpr unsigned KPBYTES = (unsigned)B_ * HKV * NG * D * 2u, VPBYTES = (unsigned)B_ * HKV * SKV * 2u;
constexpr int L_KS = 0, L_KZ = 4096, L_VS = 8192, L_VZ = 10240, L_Y = 12288, L_M = L_Y + WAVES * 4096, L_T = L_M + 256;
constexpr int TROW = 272, TSZ = 16 * TROW;
constexpr int L_END = L_T + WAVES * TSZ;
constexpr unsigned WS_EP = 0, WS_ML = 16384, WS_REC = WS_ML + NBH * REP * 1024, WS_END = WS_REC + NBH * REP * 8192;

typedef int i32x4 __attribute__((ext_vector_type(4)));
typedef unsigned u32x4 __attribute__((ext_vector_type(4)));
typedef unsigned u32x2 __attribute__((ext_vector_type(2)));
typedef float f32x4 __attribute__((ext_vector_type(4)));
typedef float f32x2 __attribute__((ext_vector_type(2)));
typedef float f32x16 __attribute__((ext_vector_type(16)));
typedef __bf16 bf16x8 __attribute__((ext_vector_type(8)));
typedef __bf16 bf16x2 __attribute__((ext_vector_type(2)));

__device__ __forceinline__ unsigned pk_bf16(float lo, float hi) {
  f32x2 v = {lo, hi};
  return __builtin_bit_cast(unsigned, __builtin_convertvector(v, bf16x2));
}
__device__ __forceinline__ float rnd_bf16(float x) { return __builtin_bit_cast(float, pk_bf16(0.f, x)); }
__device__ __forceinline__ float bf_lo(unsigned u) { return __builtin_bit_cast(float, u << 16); }
__device__ __forceinline__ float bf_hi(unsigned u) { return __builtin_bit_cast(float, u & 0xffff0000u); }
__device__ __forceinline__ f32x4 mfma16(u32x4 a, u32x4 b, f32x4 c) {
  return __builtin_amdgcn_mfma_f32_16x16x32_bf16(__builtin_bit_cast(bf16x8, a), __builtin_bit_cast(bf16x8, b), c, 0, 0, 0);
}
__device__ __forceinline__ f32x16 mfma32(u32x4 a, u32x4 b, f32x16 c) {
  return __builtin_amdgcn_mfma_f32_32x32x16_bf16(__builtin_bit_cast(bf16x8, a), __builtin_bit_cast(bf16x8, b), c, 0, 0, 0);
}
__device__ __forceinline__ float xg_max(float x) {
  auto s = __builtin_amdgcn_permlane16_swap(__float_as_uint(x), __float_as_uint(x), false, false);
  x = fmaxf(__uint_as_float(s[0]), __uint_as_float(s[1]));
  auto t = __builtin_amdgcn_permlane32_swap(__float_as_uint(x), __float_as_uint(x), false, false);
  return fmaxf(__uint_as_float(t[0]), __uint_as_float(t[1]));
}
__device__ __forceinline__ float xg_sum(float x) {
  auto s = __builtin_amdgcn_permlane16_swap(__float_as_uint(x), __float_as_uint(x), false, false);
  x = __uint_as_float(s[0]) + __uint_as_float(s[1]);
  auto t = __builtin_amdgcn_permlane32_swap(__float_as_uint(x), __float_as_uint(x), false, false);
  return __uint_as_float(t[0]) + __uint_as_float(t[1]);
}
__device__ __forceinline__ float ror8(float x) {
  return __builtin_bit_cast(float, __builtin_amdgcn_mov_dpp(__builtin_bit_cast(int, x), 0x128, 0xf, 0xf, true));
}
__device__ __forceinline__ __amdgpu_buffer_rsrc_t mkrsrc(const void* p, unsigned bytes) {
  return __builtin_amdgcn_make_buffer_rsrc(const_cast<void*>(p), 0, (int)bytes, 0x00020000);
}
__device__ __forceinline__ unsigned now32() {
  unsigned long long t; asm volatile("s_memrealtime %0\n\ts_waitcnt lgkmcnt(0)" : "=s"(t) :: "memory"); return (unsigned)t;
}
__device__ __forceinline__ unsigned group_of(unsigned sp, unsigned lg) {
  return (lg & 1) | ((sp & 1) << 1) | ((lg & 14) << 1) | ((sp & 6) << 4);
}
__device__ __forceinline__ unsigned local_group(unsigned step, unsigned bl) {
  return (step + 14 * bl) & 15;
}
#define SB() __builtin_amdgcn_sched_barrier(0)
#define ST(k)

__global__ void __launch_bounds__(NTHR, 2)
kv_decode(const uint16_t* __restrict__ q, const int* __restrict__ qk, const int* __restrict__ qv,
          const uint16_t* __restrict__ ks, const uint16_t* __restrict__ kz,
          const uint16_t* __restrict__ vs, const uint16_t* __restrict__ vz,
          unsigned* ws, unsigned* out) {
  __shared__ __attribute__((aligned(16))) char lds[L_END];
  ST(0);
  const unsigned tstart = (unsigned)__builtin_amdgcn_s_memrealtime();
  asm volatile("" :: "s"(q), "s"(qk), "s"(qv), "s"(ks), "s"(kz), "s"(vs), "s"(vz), "s"(ws), "s"(out), "s"(tstart));
  const unsigned split = (blockIdx.x >> 3) & 7, bl = blockIdx.x >> 6, bh = (blockIdx.x & 7) * 4 + bl;
  const int tid = threadIdx.x, lane = tid & 63;
  const unsigned wave = __builtin_amdgcn_readfirstlane(tid >> 6);
  const int c = lane & 15, g4 = lane >> 4, hh = lane >> 5, ch = lane & 31;
  const __amdgpu_buffer_rsrc_t rws = mkrsrc(ws, WS_END);

  const unsigned ko = group_of(split, tid >> 4) * 256 + (tid & 15) * 16;
  const u32x4 st0 = __builtin_amdgcn_raw_buffer_load_b128(mkrsrc(ks, KPBYTES), ko, bh * (NG * D * 2), 0);
  const u32x4 st1 = __builtin_amdgcn_raw_buffer_load_b128(mkrsrc(kz, KPBYTES), ko, bh * (NG * D * 2), 0);
  const u32x4 st2 = __builtin_amdgcn_raw_buffer_load_b128(mkrsrc(wave < 2 ? vs : vz, VPBYTES),
                                                          group_of(split, (tid & 127) >> 3) * 128 + (tid & 7) * 16, bh * (SKV * 2), 0);
  const unsigned row = bh * REP + wave;
  const unsigned rowc = ((bh + CSHIFT) & (NBH - 1)) * REP + wave;
  const unsigned epp = __builtin_amdgcn_raw_buffer_load_b32(rws, (lane >> 3) * 4, WS_EP + row * 32, 16);
  const unsigned epc = __builtin_amdgcn_raw_buffer_load_b32(rws, 0, WS_EP + rowc * 32 + split * 4, 16);
  u32x4 Bq[4];
#pragma unroll
  for (int m = 0; m < 4; ++m)
    Bq[m] = __builtin_amdgcn_raw_buffer_load_b128(mkrsrc(q, B_ * HQ * D * 2), (c & 3) * 256 + g4 * 16 + m * 64, bh * 1024, 0);
  SB();

  const unsigned vo0 = lane * 16, vo1 = lane * 16 + 4096;
  u32x4 buf[8];
  auto tile_off = [&](unsigned lg) -> unsigned { return ((bh * NG + group_of(split, lg)) * 4 + wave) * 8192u; };
  auto issue = [&](int n, const int* base, unsigned bytes, unsigned so) __attribute__((always_inline)) {
    buf[n] = __builtin_amdgcn_raw_buffer_load_b128(mkrsrc(base, bytes), (n < 4 ? vo0 : vo1) + (n & 3) * 1024, so, 2);
  };
  unsigned lg = local_group(0, bl), so_cur = tile_off(lg);
#pragma unroll
  for (int i = 0; i < WIN; ++i) issue(i, qk, KVBYTES, so_cur);
  SB();
  ST(2);
  *reinterpret_cast<u32x4*>(lds + L_KS + tid * 16) = st0;
  *reinterpret_cast<u32x4*>(lds + L_KZ + tid * 16) = st1;
  *reinterpret_cast<u32x4*>(lds + L_VS + tid * 16) = st2;
  const unsigned tagp = epp + 0x9E3779B9u, tag = __builtin_amdgcn_readfirstlane(epc) + 0x9E3779B9u;
  __syncthreads();

  char* const tw = lds + L_T + wave * TSZ + hh * 8 * TROW + ch * 8;
  const char* const tr = lds + L_T + wave * TSZ + c * TROW + g4 * 16;
  char* const yb = lds + L_Y + wave * 4096 + (g4 * 4 + (c & 3)) * 16;
  constexpr float SCALE = 0.088388347648318440f, LOG2E = 1.4426950408889634f;
  unsigned ahead = 0;
  auto check = [&](int idx) __attribute__((always_inline)) {
    ahead = (int)((now32() - tstart - TOFF) * 256u - (unsigned)(idx - WLO) * TSCH) < 0;
  };
#define THROTTLE(LO, HI) asm volatile("s_waitcnt vmcnt(%2)\n\ts_cmp_eq_u32 %0, 0\n\ts_cbranch_scc1 1f\n\ts_waitcnt vmcnt(%1)\n1:" :: "s"(ahead), "n"((LO) - 1), "n"((HI) - 1) : "scc", "memory")

  float mx = -INFINITY;
#pragma clang loop unroll(disable)
  for (unsigned it = 0; it < NST; ++it) {
    const u32x2 sraw = *reinterpret_cast<const u32x2*>(lds + L_KS + lg * 256 + ch * 8);
    const u32x2 zraw = *reinterpret_cast<const u32x2*>(lds + L_KZ + lg * 256 + ch * 8);
    const f32x2 s01 = {bf_lo(sraw[0]), bf_hi(sraw[0])}, s23 = {bf_lo(sraw[1]), bf_hi(sraw[1])};
    const f32x2 z01 = {bf_lo(zraw[0]), bf_hi(zraw[0])}, z23 = {bf_lo(zraw[1]), bf_hi(zraw[1])};
    lg = local_group(it + 1, bl);
    const unsigned so_nxt = tile_off(lg);
    const int* const nb = it + 1 < NST ? qk : qv;
    SB();
#pragma unroll
    for (int n = 0; n < 8; ++n) {
      const i32x4 a = __builtin_bit_cast(i32x4, buf[n]);
      f32x2 t01 = {(float)a[0], (float)a[1]}, t23 = {(float)a[2], (float)a[3]};
      asm volatile("" : "+v"(t01), "+v"(t23));
      SB();
      { const int j = n + WIN; if ((n & 3) == 0) check(it * 8 + j); THROTTLE(WLO, WHI1);
        if (j < 8) issue(j, qk, KVBYTES, so_cur); else issue(j - 8, nb, KVBYTES, so_nxt); }
      SB();
      t01 -= z01; t23 -= z23;
      t01 = f32x2{rnd_bf16(t01[0]), rnd_bf16(t01[1])} * s01;
      t23 = f32x2{rnd_bf16(t23[0]), rnd_bf16(t23[1])} * s23;
      *reinterpret_cast<u32x2*>(tw + n * TROW) = u32x2{pk_bf16(t01[0], t01[1]), pk_bf16(t23[0], t23[1])};
    }
    asm volatile("" ::: "memory");
    f32x4 S = {0.f, 0.f, 0.f, 0.f};
#pragma unroll
    for (int m = 0; m < 4; ++m) S = mfma16(*reinterpret_cast<const u32x4*>(tr + m * 64), Bq[m], S);
    asm volatile("" ::: "memory");
    f32x4 y;
#pragma unroll
    for (int r = 0; r < 4; ++r) y[r] = rnd_bf16(rnd_bf16(S[r]) * SCALE);
    mx = fmaxf(fmaxf(mx, fmaxf(y[0], y[1])), fmaxf(y[2], y[3]));
    *reinterpret_cast<f32x4*>(yb + it * 256) = y;
    so_cur = so_nxt;
  }
  ST(1);
  mx = xg_max(mx);
  if (lane < 4) *reinterpret_cast<float*>(lds + L_M + (lane * 4 + wave) * 4) = mx;
  __syncthreads();
  {
    const f32x4 m4 = *reinterpret_cast<const f32x4*>(lds + L_M + (c & 3) * 16);
    mx = fmaxf(fmaxf(m4[0], m4[1]), fmaxf(m4[2], m4[3]));
  }

  const bool lo_lane = (c >> 2) & 1;
  const unsigned sel = hh ? 0x03020c0cu : 0x01000c0cu;
  float l_run = 0.f, z_run = 0.f;
  f32x16 acc[4];
#pragma unroll
  for (int cc = 0; cc < 4; ++cc)
#pragma unroll
    for (int r = 0; r < 16; ++r) acc[cc][r] = 0.f;
  float* const rec = reinterpret_cast<float*>(lds + L_T + wave * TSZ);
#pragma clang loop unroll(disable)
  for (unsigned it = 0; it < NST; ++it) {
    const f32x4 y = *reinterpret_cast<const f32x4*>(yb + it * 256);
    const u32x4 vsr = *reinterpret_cast<const u32x4*>(lds + L_VS + lg * 128 + wave * 32 + (g4 & 1) * 16);
    const u32x4 vzr = *reinterpret_cast<const u32x4*>(lds + L_VZ + lg * 128 + wave * 32 + (g4 & 1) * 16);
    lg = local_group(it + 1, bl);
    const unsigned so_nxt = tile_off(lg);
    const unsigned nbytes = it + 1 < NST ? KVBYTES : 0u;
    u32x4 Bv[4];
    auto vpair = [&](int u) __attribute__((always_inline)) {
      SB();
      const i32x4 a = __builtin_bit_cast(i32x4, buf[2 * u]);
      f32x4 fa = {(float)a[0], (float)a[1], (float)a[2], (float)a[3]};
      asm volatile("" : "+v"(fa));
      SB();
      { const int j = 2 * u + WIN; if ((u & 1) == 0) { check(128 + it * 8 + j); ahead = nbytes ? ahead : 0u; } THROTTLE(WLO2, WHI2);
        if (j < 8) issue(j, qv, KVBYTES, so_cur); else issue(j - 8, qv, nbytes, so_nxt); }
      SB();
      const i32x4 b = __builtin_bit_cast(i32x4, buf[2 * u + 1]);
#pragma unroll
      for (int cc = 0; cc < 4; ++cc) Bv[cc][u] = pk_bf16(fa[cc], (float)b[cc]);
      SB();
      { const int j = 2 * u + 1 + WIN; THROTTLE(WLO2, WHI2);
        if (j < 8) issue(j, qv, KVBYTES, so_cur); else issue(j - 8, qv, nbytes, so_nxt); }
      SB();
    };
    vpair(0);
    float p[4];
#pragma unroll
    for (int r = 0; r < 4; ++r) p[r] = __builtin_amdgcn_exp2f((y[r] - mx) * LOG2E);
    l_run += (p[0] + p[1]) + (p[2] + p[3]);
    float pv[4];
#pragma unroll
    for (int r = 0; r < 4; ++r) {
      pv[r] = p[r] * __builtin_bit_cast(float, __builtin_amdgcn_perm(vsr[r], vsr[r], sel));
      z_run = fmaf(pv[r], __builtin_bit_cast(float, __builtin_amdgcn_perm(vzr[r], vzr[r], sel)), z_run);
    }
    vpair(1);
    unsigned X[2];
#pragma unroll
    for (int u = 0; u < 2; ++u) {
      const unsigned h = pk_bf16(pv[2 * u], pv[2 * u + 1]);
      const unsigned l = pk_bf16(pv[2 * u] - bf_lo(h), pv[2 * u + 1] - bf_hi(h));
      X[u] = lo_lane ? l : h;
    }
    const auto w0 = __builtin_amdgcn_permlane16_swap(X[0], X[0], false, false);
    const auto w1 = __builtin_amdgcn_permlane16_swap(X[1], X[1], false, false);
    const u32x4 P = {w0[0], w1[0], w0[1], w1[1]};
    vpair(2);
    {
      const f32x2 lz = {xg_sum(l_run), xg_sum(z_run)};
      *reinterpret_cast<f32x2*>(rec + 1024 + 2 * (c & 3)) = lz;
    }
    vpair(3);
#pragma unroll
    for (int cc = 0; cc < 4; ++cc) acc[cc] = mfma32(P, Bv[cc], acc[cc]);
    so_cur = so_nxt;
  }
  ST(3);

  u32x4 g = __builtin_amdgcn_raw_buffer_load_b128(rws, lane * 16, WS_REC + rowc * 8192 + split * 1024, 16);
  u32x4 ml = __builtin_amdgcn_raw_buffer_load_b128(rws, (lane >> 3) * 16, WS_ML + rowc * 1024 + split * 128, 16);
  SB();
#pragma unroll
  for (int r = 0; r < 4; ++r)
    *reinterpret_cast<f32x4*>(rec + (hh * 4 + r) * D + 4 * ch) = f32x4{acc[0][r], acc[1][r], acc[2][r], acc[3][r]};
  const float M = __builtin_bit_cast(float, __builtin_amdgcn_readlane(__builtin_bit_cast(int, mx), wave));
  __syncthreads();
  ST(4);
  f32x2 lz[WAVES], hi[WAVES], lo[WAVES];
#pragma unroll
  for (int w = 0; w < WAVES; ++w) {
    const float* rw = reinterpret_cast<const float*>(lds + L_T + w * TSZ);
    hi[w] = *reinterpret_cast<const f32x2*>(rw + wave * D + 2 * lane);
    lo[w] = *reinterpret_cast<const f32x2*>(rw + (4 + wave) * D + 2 * lane);
    lz[w] = *reinterpret_cast<const f32x2*>(rw + 1024 + 2 * wave);
  }
  SB();
  const f32x2 LZ = (lz[0] + lz[1]) + (lz[2] + lz[3]);
  const f32x2 o = (((hi[0] + lo[0]) + (hi[1] + lo[1])) + ((hi[2] + lo[2]) + (hi[3] + lo[3]))) - LZ[1];
  __builtin_amdgcn_raw_buffer_store_b128(u32x4{__float_as_uint(o[0]), tagp, __float_as_uint(o[1]), tagp}, rws,
                                         (lane >> 3) * 1024 + (lane & 7) * 16, WS_REC + row * 8192 + split * 128, 16);
  __builtin_amdgcn_raw_buffer_store_b128(u32x4{__float_as_uint(M), tagp, __float_as_uint(LZ[0]), tagp}, rws,
                                         (lane & 7) == 0 ? lane * 16u : 0x80000000u, WS_ML + row * 1024 + split * 16, 16);
  ST(5);
  bool need = !(g[1] == tag && g[3] == tag && ml[1] == tag && ml[3] == tag);
#pragma clang loop unroll(disable)
  for (unsigned spin = 0; spin < (1u << 20); ++spin) {
    if (!__any(need)) break;
    if (need) {
      unsigned rb = rowc;
      asm volatile("" : "+s"(rb) :: "memory");
      g = __builtin_amdgcn_raw_buffer_load_b128(rws, lane * 16, WS_REC + rb * 8192 + split * 1024, 16);
      ml = __builtin_amdgcn_raw_buffer_load_b128(rws, (lane >> 3) * 16, WS_ML + rb * 1024 + split * 128, 16);
      need = !(g[1] == tag && g[3] == tag && ml[1] == tag && ml[3] == tag);
    }
  }
  ST(6);
  const float mp = __uint_as_float(ml[0]);
  const float Mx = xg_max(fmaxf(mp, ror8(mp)));
  const float f = __builtin_amdgcn_exp2f((mp - Mx) * LOG2E);
  float a0 = f * __uint_as_float(g[0]), a1 = f * __uint_as_float(g[2]), Lx = f * __uint_as_float(ml[2]);
  a0 = xg_sum(a0 + ror8(a0));
  a1 = xg_sum(a1 + ror8(a1));
  Lx = __builtin_amdgcn_rcpf(xg_sum(Lx + ror8(Lx)));
  __builtin_amdgcn_raw_buffer_store_b32(pk_bf16(a0 * Lx, a1 * Lx), mkrsrc(out, B_ * HQ * D * 2), lane < 8 ? lane * 4u : 0x80000000u, rowc * 256 + split * 32, 0);
  __builtin_amdgcn_raw_buffer_store_b32(tag, rws, lane == 0 ? 0u : 0x80000000u, WS_EP + rowc * 32 + split * 4, 0);
  ST(7);
}

extern "C" void kernel_launch(void* const* d_in, const int* in_sizes, int n_in, void* d_out, int out_size,
                              void* d_ws, size_t ws_size, hipStream_t stream) {
  kv_decode<<<NWG, NTHR, 0, stream>>>((const uint16_t*)d_in[0], (const int*)d_in[1], (const int*)d_in[2],
                                      (const uint16_t*)d_in[3], (const uint16_t*)d_in[4],
                                      (const uint16_t*)d_in[5], (const uint16_t*)d_in[6],
                                      (unsigned*)d_ws, (unsigned*)d_out);
}
```

```cpp
#define STAMP
#define QMODE 0
#define QK 7
#define AMP 0
#define SUB 0
#include <hip/hip_runtime.h>
#include <hip/hip_bf16.h>
#include <stdint.h>

#define WIN 8
#define WLO 5
#define WHI1 8
#define WLO2 4
#define WHI2 7
#define TOUCH 0
#define TSCH 3450
#define TOFF 140
#define PAT 3
#define CSHIFT 8

constexpr int B_ = 4, HQ = 32, HKV = 8, SKV = 8192, D = 128, GRP = 64, NG = SKV / GRP, REP = HQ / HKV;
constexpr int NBH = B_ * HKV, NSPLIT = 8, NWG = NBH * NSPLIT, WAVES = 4, NTHR = WAVES * 64, NST = 16;
constexpr unsigned KVBYTES = (unsigned)B_ * HKV * SKV * D * 4u;
constexpr unsigned KPBYTES = (unsigned)B_ * HKV * NG * D * 2u, VPBYTES = (unsigned)B_ * HKV * SKV * 2u;
constexpr int L_KS = 0, L_KZ = 4096, L_VS = 8192, L_VZ = 10240, L_Y = 12288, L_M = L_Y + WAVES * 4096, L_T = L_M + 256;
constexpr int TROW = 272, TSZ = 16 * TROW;
constexpr int L_END = L_T + WAVES * TSZ;
constexpr unsigned WS_EP = 0, WS_ML = 16384, WS_REC = WS_ML + NBH * REP * 1024, WS_END = WS_REC + NBH * REP * 8192;

typedef int i32x4 __attribute__((ext_vector_type(4)));
typedef unsigned u32x4 __attribute__((ext_vector_type(4)));
typedef unsigned u32x2 __attribute__((ext_vector_type(2)));
typedef float f32x4 __attribute__((ext_vector_type(4)));
typedef float f32x2 __attribute__((ext_vector_type(2)));
typedef float f32x16 __attribute__((ext_vector_type(16)));
typedef __bf16 bf16x8 __attribute__((ext_vector_type(8)));
typedef __bf16 bf16x2 __attribute__((ext_vector_type(2)));

__device__ __forceinline__ unsigned pk_bf16(float lo, float hi) {
  f32x2 v = {lo, hi};
  return __builtin_bit_cast(unsigned, __builtin_convertvector(v, bf16x2));
}
__device__ __forceinline__ float rnd_bf16(float x) { return __builtin_bit_cast(float, pk_bf16(0.f, x)); }
__device__ __forceinline__ float bf_lo(unsigned u) { return __builtin_bit_cast(float, u << 16); }
__device__ __forceinline__ float bf_hi(unsigned u) { return __builtin_bit_cast(float, u & 0xffff0000u); }
__device__ __forceinline__ f32x4 mfma16(u32x4 a, u32x4 b, f32x4 c) {
  return __builtin_amdgcn_mfma_f32_16x16x32_bf16(__builtin_bit_cast(bf16x8, a), __builtin_bit_cast(bf16x8, b), c, 0, 0, 0);
}
__device__ __forceinline__ f32x16 mfma32(u32x4 a, u32x4 b, f32x16 c) {
  return __builtin_amdgcn_mfma_f32_32x32x16_bf16(__builtin_bit_cast(bf16x8, a), __builtin_bit_cast(bf16x8, b), c, 0, 0, 0);
}
__device__ __forceinline__ float xg_max(float x) {
  auto s = __builtin_amdgcn_permlane16_swap(__float_as_uint(x), __float_as_uint(x), false, false);
  x = fmaxf(__uint_as_float(s[0]), __uint_as_float(s[1]));
  auto t = __builtin_amdgcn_permlane32_swap(__float_as_uint(x), __float_as_uint(x), false, false);
  return fmaxf(__uint_as_float(t[0]), __uint_as_float(t[1]));
}
__device__ __forceinline__ float xg_sum(float x) {
  auto s = __builtin_amdgcn_permlane16_swap(__float_as_uint(x), __float_as_uint(x), false, false);
  x = __uint_as_float(s[0]) + __uint_as_float(s[1]);
  auto t = __builtin_amdgcn_permlane32_swap(__float_as_uint(x), __float_as_uint(x), false, false);
  return __uint_as_float(t[0]) + __uint_as_float(t[1]);
}
__device__ __forceinline__ float ror8(float x) {
  return __builtin_bit_cast(float, __builtin_amdgcn_mov_dpp(__builtin_bit_cast(int, x), 0x128, 0xf, 0xf, true));
}
__device__ __forceinline__ __amdgpu_buffer_rsrc_t mkrsrc(const void* p, unsigned bytes) {
  return __builtin_amdgcn_make_buffer_rsrc(const_cast<void*>(p), 0, (int)bytes, 0x00020000);
}
__device__ __forceinline__ unsigned now32() {
  unsigned long long t; asm volatile("s_memrealtime %0\n\ts_waitcnt lgkmcnt(0)" : "=s"(t) :: "memory"); return (unsigned)t;
}
__device__ __forceinline__ unsigned group_of(unsigned sp, unsigned lg) {
  return (lg & 1) | ((sp & 1) << 1) | ((lg & 14) << 1) | ((sp & 6) << 4);
}
__device__ __forceinline__ unsigned local_group(unsigned step, unsigned bl) {
  return (step + 14 * bl) & 15;
}
#define SB() __builtin_amdgcn_sched_barrier(0)
#define NSTAMP 8
#define STAMP_WAVES (NWG * WAVES)
#define STAMP_OFF (4u << 20)
typedef unsigned long long u64s;
__device__ __forceinline__ u64s rt() { u64s t; asm volatile("s_memrealtime %0\n\ts_waitcnt lgkmcnt(0)" : "=s"(t) :: "memory"); return t; }
#define ST(k) do { SB(); stamp[k] = rt(); SB(); } while (0)

__global__ void __launch_bounds__(NTHR, 2)
kv_decode(const uint16_t* __restrict__ q, const int* __restrict__ qk, const int* __restrict__ qv,
          const uint16_t* __restrict__ ks, const uint16_t* __restrict__ kz,
          const uint16_t* __restrict__ vs, const uint16_t* __restrict__ vz,
          unsigned* ws, unsigned* out) {
  __shared__ __attribute__((aligned(16))) char lds[L_END];
  u64s stamp[NSTAMP] = {0, 0, 0, 0, 0, 0, 0, 0};
  ST(0);
  const unsigned tstart = (unsigned)__builtin_amdgcn_s_memrealtime();
  asm volatile("" :: "s"(q), "s"(qk), "s"(qv), "s"(ks), "s"(kz), "s"(vs), "s"(vz), "s"(ws), "s"(out), "s"(tstart));
  const unsigned split = (blockIdx.x >> 3) & 7, bl = blockIdx.x >> 6, bh = (blockIdx.x & 7) * 4 + bl;
  const int tid = threadIdx.x, lane = tid & 63;
  const unsigned wave = __builtin_amdgcn_readfirstlane(tid >> 6);
  const int c = lane & 15, g4 = lane >> 4, hh = lane >> 5, ch = lane & 31;
  const __amdgpu_buffer_rsrc_t rws = mkrsrc(ws, WS_END);

  const unsigned ko = group_of(split, tid >> 4) * 256 + (tid & 15) * 16;
  const u32x4 st0 = __builtin_amdgcn_raw_buffer_load_b128(mkrsrc(ks, KPBYTES), ko, bh * (NG * D * 2), 0);
  const u32x4 st1 = __builtin_amdgcn_raw_buffer_load_b128(mkrsrc(kz, KPBYTES), ko, bh * (NG * D * 2), 0);
  const u32x4 st2 = __builtin_amdgcn_raw_buffer_load_b128(mkrsrc(wave < 2 ? vs : vz, VPBYTES),
                                                          group_of(split, (tid & 127) >> 3) * 128 + (tid & 7) * 16, bh * (SKV * 2), 0);
  const unsigned row = bh * REP + wave;
  const unsigned rowc = ((bh + CSHIFT) & (NBH - 1)) * REP + wave;
  const unsigned epp = __builtin_amdgcn_raw_buffer_load_b32(rws, (lane >> 3) * 4, WS_EP + row * 32, 16);
  const unsigned epc = __builtin_amdgcn_raw_buffer_load_b32(rws, 0, WS_EP + rowc * 32 + split * 4, 16);
  u32x4 Bq[4];
#pragma unroll
  for (int m = 0; m < 4; ++m)
    Bq[m] = __builtin_amdgcn_raw_buffer_load_b128(mkrsrc(q, B_ * HQ * D * 2), (c & 3) * 256 + g4 * 16 + m * 64, bh * 1024, 0);
  SB();

  const unsigned vo0 = lane * 16, vo1 = lane * 16 + 4096;
  u32x4 buf[8];
  auto tile_off = [&](unsigned lg) -> unsigned { return ((bh * NG + group_of(split, lg)) * 4 + wave) * 8192u; };
  auto issue = [&](int n, const int* base, unsigned bytes, unsigned so) __attribute__((always_inline)) {
    buf[n] = __builtin_amdgcn_raw_buffer_load_b128(mkrsrc(base, bytes), (n < 4 ? vo0 : vo1) + (n & 3) * 1024, so, 2);
  };
  unsigned lg = local_group(0, bl), so_cur = tile_off(lg);
#pragma unroll
  for (int i = 0; i < WIN; ++i) issue(i, qk, KVBYTES, so_cur);
  SB();
  ST(2);
  *reinterpret_cast<u32x4*>(lds + L_KS + tid * 16) = st0;
  *reinterpret_cast<u32x4*>(lds + L_KZ + tid * 16) = st1;
  *reinterpret_cast<u32x4*>(lds + L_VS + tid * 16) = st2;
  const unsigned tagp = epp + 0x9E3779B9u, tag = __builtin_amdgcn_readfirstlane(epc) + 0x9E3779B9u;
  __syncthreads();

  char* const tw = lds + L_T + wave * TSZ + hh * 8 * TROW + ch * 8;
  const char* const tr = lds + L_T + wave * TSZ + c * TROW + g4 * 16;
  char* const yb = lds + L_Y + wave * 4096 + (g4 * 4 + (c & 3)) * 16;
  constexpr float SCALE = 0.088388347648318440f, LOG2E = 1.4426950408889634f;
  unsigned ahead = 0;
  auto check = [&](int idx) __attribute__((always_inline)) {
    ahead = (int)((now32() - tstart - TOFF) * 256u - (unsigned)(idx - WLO) * TSCH) < 0;
  };
#define THROTTLE(LO, HI) asm volatile("s_waitcnt vmcnt(%2)\n\ts_cmp_eq_u32 %0, 0\n\ts_cbranch_scc1 1f\n\ts_waitcnt vmcnt(%1)\n1:" :: "s"(ahead), "n"((LO) - 1), "n"((HI) - 1) : "scc", "memory")

  float mx = -INFINITY;
#pragma clang loop unroll(disable)
  for (unsigned it = 0; it < NST; ++it) {
    const u32x2 sraw = *reinterpret_cast<const u32x2*>(lds + L_KS + lg * 256 + ch * 8);
    const u32x2 zraw = *reinterpret_cast<const u32x2*>(lds + L_KZ + lg * 256 + ch * 8);
    const f32x2 s01 = {bf_lo(sraw[0]), bf_hi(sraw[0])}, s23 = {bf_lo(sraw[1]), bf_hi(sraw[1])};
    const f32x2 z01 = {bf_lo(zraw[0]), bf_hi(zraw[0])}, z23 = {bf_lo(zraw[1]), bf_hi(zraw[1])};
    lg = local_group(it + 1, bl);
    const unsigned so_nxt = tile_off(lg);
    const int* const nb = it + 1 < NST ? qk : qv;
    SB();
#pragma unroll
    for (int n = 0; n < 8; ++n) {
      const i32x4 a = __builtin_bit_cast(i32x4, buf[n]);
      f32x2 t01 = {(float)a[0], (float)a[1]}, t23 = {(float)a[2], (float)a[3]};
      asm volatile("" : "+v"(t01), "+v"(t23));
      SB();
      { const int j = n + WIN; if ((n & 3) == 0) check(it * 8 + j); THROTTLE(WLO, WHI1);
        if (j < 8) issue(j, qk, KVBYTES, so_cur); else issue(j - 8, nb, KVBYTES, so_nxt); }
      SB();
      t01 -= z01; t23 -= z23;
      t01 = f32x2{rnd_bf16(t01[0]), rnd_bf16(t01[1])} * s01;
      t23 = f32x2{rnd_bf16(t23[0]), rnd_bf16(t23[1])} * s23;
      *reinterpret_cast<u32x2*>(tw + n * TROW) = u32x2{pk_bf16(t01[0], t01[1]), pk_bf16(t23[0], t23[1])};
    }
    asm volatile("" ::: "memory");
    f32x4 S = {0.f, 0.f, 0.f, 0.f};
#pragma unroll
    for (int m = 0; m < 4; ++m) S = mfma16(*reinterpret_cast<const u32x4*>(tr + m * 64), Bq[m], S);
    asm volatile("" ::: "memory");
    f32x4 y;
#pragma unroll
    for (int r = 0; r < 4; ++r) y[r] = rnd_bf16(rnd_bf16(S[r]) * SCALE);
    mx = fmaxf(fmaxf(mx, fmaxf(y[0], y[1])), fmaxf(y[2], y[3]));
    *reinterpret_cast<f32x4*>(yb + it * 256) = y;
    so_cur = so_nxt;
  }
  ST(1);
  mx = xg_max(mx);
  if (lane < 4) *reinterpret_cast<float*>(lds + L_M + (lane * 4 + wave) * 4) = mx;
  __syncthreads();
  {
    const f32x4 m4 = *reinterpret_cast<const f32x4*>(lds + L_M + (c & 3) * 16);
    mx = fmaxf(fmaxf(m4[0], m4[1]), fmaxf(m4[2], m4[3]));
  }

  const bool lo_lane = (c >> 2) & 1;
  const unsigned sel = hh ? 0x03020c0cu : 0x01000c0cu;
  float l_run = 0.f, z_run = 0.f;
  f32x16 acc[4];
#pragma unroll
  for (int cc = 0; cc < 4; ++cc)
#pragma unroll
    for (int r = 0; r < 16; ++r) acc[cc][r] = 0.f;
  float* const rec = reinterpret_cast<float*>(lds + L_T + wave * TSZ);
#pragma clang loop unroll(disable)
  for (unsigned it = 0; it < NST; ++it) {
    const f32x4 y = *reinterpret_cast<const f32x4*>(yb + it * 256);
    const u32x4 vsr = *reinterpret_cast<const u32x4*>(lds + L_VS + lg * 128 + wave * 32 + (g4 & 1) * 16);
    const u32x4 vzr = *reinterpret_cast<const u32x4*>(lds + L_VZ + lg * 128 + wave * 32 + (g4 & 1) * 16);
    lg = local_group(it + 1, bl);
    const unsigned so_nxt = tile_off(lg);
    const unsigned nbytes = it + 1 < NST ? KVBYTES : 0u;
    u32x4 Bv[4];
    auto vpair = [&](int u) __attribute__((always_inline)) {
      SB();
      const i32x4 a = __builtin_bit_cast(i32x4, buf[2 * u]);
      f32x4 fa = {(float)a[0], (float)a[1], (float)a[2], (float)a[3]};
      asm volatile("" : "+v"(fa));
      SB();
      { const int j = 2 * u + WIN; if ((u & 1) == 0) { check(128 + it * 8 + j); ahead = nbytes ? ahead : 0u; } THROTTLE(WLO2, WHI2);
        if (j < 8) issue(j, qv, KVBYTES, so_cur); else issue(j - 8, qv, nbytes, so_nxt); }
      SB();
      const i32x4 b = __builtin_bit_cast(i32x4, buf[2 * u + 1]);
#pragma unroll
      for (int cc = 0; cc < 4; ++cc) Bv[cc][u] = pk_bf16(fa[cc], (float)b[cc]);
      SB();
      { const int j = 2 * u + 1 + WIN; THROTTLE(WLO2, WHI2);
        if (j < 8) issue(j, qv, KVBYTES, so_cur); else issue(j - 8, qv, nbytes, so_nxt); }
      SB();
    };
    vpair(0);
    float p[4];
#pragma unroll
    for (int r = 0; r < 4; ++r) p[r] = __builtin_amdgcn_exp2f((y[r] - mx) * LOG2E);
    l_run += (p[0] + p[1]) + (p[2] + p[3]);
    float pv[4];
#pragma unroll
    for (int r = 0; r < 4; ++r) {
      pv[r] = p[r] * __builtin_bit_cast(float, __builtin_amdgcn_perm(vsr[r], vsr[r], sel));
      z_run = fmaf(pv[r], __builtin_bit_cast(float, __builtin_amdgcn_perm(vzr[r], vzr[r], sel)), z_run);
    }
    vpair(1);
    unsigned X[2];
#pragma unroll
    for (int u = 0; u < 2; ++u) {
      const unsigned h = pk_bf16(pv[2 * u], pv[2 * u + 1]);
      const unsigned l = pk_bf16(pv[2 * u] - bf_lo(h), pv[2 * u + 1] - bf_hi(h));
      X[u] = lo_lane ? l : h;
    }
    const auto w0 = __builtin_amdgcn_permlane16_swap(X[0], X[0], false, false);
    const auto w1 = __builtin_amdgcn_permlane16_swap(X[1], X[1], false, false);
    const u32x4 P = {w0[0], w1[0], w0[1], w1[1]};
    vpair(2);
    {
      const f32x2 lz = {xg_sum(l_run), xg_sum(z_run)};
      *reinterpret_cast<f32x2*>(rec + 1024 + 2 * (c & 3)) = lz;
    }
    vpair(3);
#pragma unroll
    for (int cc = 0; cc < 4; ++cc) acc[cc] = mfma32(P, Bv[cc], acc[cc]);
    so_cur = so_nxt;
  }
  ST(3);

  u32x4 g = __builtin_amdgcn_raw_buffer_load_b128(rws, lane * 16, WS_REC + rowc * 8192 + split * 1024, 16);
  u32x4 ml = __builtin_amdgcn_raw_buffer_load_b128(rws, (lane >> 3) * 16, WS_ML + rowc * 1024 + split * 128, 16);
  SB();
#pragma unroll
  for (int r = 0; r < 4; ++r)
    *reinterpret_cast<f32x4*>(rec + (hh * 4 + r) * D + 4 * ch) = f32x4{acc[0][r], acc[1][r], acc[2][r], acc[3][r]};
  const float M = __builtin_bit_cast(float, __builtin_amdgcn_readlane(__builtin_bit_cast(int, mx), wave));
  __syncthreads();
  ST(4);
  f32x2 lz[WAVES], hi[WAVES], lo[WAVES];
#pragma unroll
  for (int w = 0; w < WAVES; ++w) {
    const float* rw = reinterpret_cast<const float*>(lds + L_T + w * TSZ);
    hi[w] = *reinterpret_cast<const f32x2*>(rw + wave * D + 2 * lane);
    lo[w] = *reinterpret_cast<const f32x2*>(rw + (4 + wave) * D + 2 * lane);
    lz[w] = *reinterpret_cast<const f32x2*>(rw + 1024 + 2 * wave);
  }
  SB();
  const f32x2 LZ = (lz[0] + lz[1]) + (lz[2] + lz[3]);
  const f32x2 o = (((hi[0] + lo[0]) + (hi[1] + lo[1])) + ((hi[2] + lo[2]) + (hi[3] + lo[3]))) - LZ[1];
  __builtin_amdgcn_raw_buffer_store_b128(u32x4{__float_as_uint(o[0]), tagp, __float_as_uint(o[1]), tagp}, rws,
                                         (lane >> 3) * 1024 + (lane & 7) * 16, WS_REC + row * 8192 + split * 128, 16);
  __builtin_amdgcn_raw_buffer_store_b128(u32x4{__float_as_uint(M), tagp, __float_as_uint(LZ[0]), tagp}, rws,
                                         (lane & 7) == 0 ? lane * 16u : 0x80000000u, WS_ML + row * 1024 + split * 16, 16);
  ST(5);
  bool need = !(g[1] == tag && g[3] == tag && ml[1] == tag && ml[3] == tag);
#pragma clang loop unroll(disable)
  for (unsigned spin = 0; spin < (1u << 20); ++spin) {
    if (!__any(need)) break;
    if (need) {
      unsigned rb = rowc;
      asm volatile("" : "+s"(rb) :: "memory");
      g = __builtin_amdgcn_raw_buffer_load_b128(rws, lane * 16, WS_REC + rb * 8192 + split * 1024, 16);
      ml = __builtin_amdgcn_raw_buffer_load_b128(rws, (lane >> 3) * 16, WS_ML + rb * 1024 + split * 128, 16);
      need = !(g[1] == tag && g[3] == tag && ml[1] == tag && ml[3] == tag);
    }
  }
  ST(6);
  const float mp = __uint_as_float(ml[0]);
  const float Mx = xg_max(fmaxf(mp, ror8(mp)));
  const float f = __builtin_amdgcn_exp2f((mp - Mx) * LOG2E);
  float a0 = f * __uint_as_float(g[0]), a1 = f * __uint_as_float(g[2]), Lx = f * __uint_as_float(ml[2]);
  a0 = xg_sum(a0 + ror8(a0));
  a1 = xg_sum(a1 + ror8(a1));
  Lx = __builtin_amdgcn_rcpf(xg_sum(Lx + ror8(Lx)));
  __builtin_amdgcn_raw_buffer_store_b32(pk_bf16(a0 * Lx, a1 * Lx), mkrsrc(out, B_ * HQ * D * 2), lane < 8 ? lane * 4u : 0x80000000u, rowc * 256 + split * 32, 0);
  __builtin_amdgcn_raw_buffer_store_b32(tag, rws, lane == 0 ? 0u : 0x80000000u, WS_EP + rowc * 32 + split * 4, 0);
  ST(7);
  if (lane == 0) {
    u64s* p = reinterpret_cast<u64s*>(reinterpret_cast<char*>(ws) + STAMP_OFF) + (size_t)((bh * NSPLIT + split) * WAVES + wave) * NSTAMP;
#pragma unroll
    for (int k = 0; k < 6; ++k) p[k] = stamp[k];
    u64s* pc = reinterpret_cast<u64s*>(reinterpret_cast<char*>(ws) + STAMP_OFF) + (size_t)((((bh + CSHIFT) & (NBH - 1)) * NSPLIT + split) * WAVES + wave) * NSTAMP;
    pc[6] = stamp[6]; pc[7] = stamp[7];
  }
}

__global__ void __launch_bounds__(1024) probe_k(const unsigned* ws, const int* qk, const int* qv) {
  __shared__ u64s smin[1024], smax[1024], ssum[1024];
  const u64s t0 = rt();
  const u64s* p = reinterpret_cast<const u64s*>(reinterpret_cast<const char*>(ws) + STAMP_OFF);
  const int t = threadIdx.x;
  const u64s a0 = p[t * NSTAMP + 0], ak = p[t * NSTAMP + QK];
  smin[t] = a0; smax[t] = ak; ssum[t] = ak;
  __syncthreads();
  for (int o = 512; o; o >>= 1) { if (t < o) { smin[t] = min(smin[t], smin[t + o]); smax[t] = max(smax[t], smax[t + o]); ssum[t] += ssum[t + o]; } __syncthreads(); }
  u64s x;
  if (QMODE == 0) x = smax[0] - smin[0] - SUB;
  else if (QMODE == 1) x = ssum[0] / 1024 - smin[0] - SUB;
  else {
    const unsigned a = __builtin_ctzll((u64s)qk | (1ull << 30)), b = __builtin_ctzll((u64s)qv | (1ull << 30));
    x = QMODE == 4 ? a * 300 : b * 300;
  }
  if (x > 20000) x = 20000;
  const u64s until = t0 + 300 + x * AMP;
  while (rt() < until) __builtin_amdgcn_s_sleep(1);
}

extern "C" int stamp_waves() { return STAMP_WAVES; }
extern "C" void kernel_launch(void* const* d_in, const int* in_sizes, int n_in, void* d_out, int out_size,
                              void* d_ws, size_t ws_size, hipStream_t stream) {
  kv_decode<<<NWG, NTHR, 0, stream>>>((const uint16_t*)d_in[0], (const int*)d_in[1], (const int*)d_in[2],
                                      (const uint16_t*)d_in[3], (const uint16_t*)d_in[4],
                                      (const uint16_t*)d_in[5], (const uint16_t*)d_in[6],
                                      (unsigned*)d_ws, (unsigned*)d_out);
  probe_k<<<1, 1024, 0, stream>>>((const unsigned*)d_ws, (const int*)d_in[1], (const int*)d_in[2]);
}
```

```cpp
#include <hip/hip_runtime.h>
#include <hip/hip_bf16.h>
#include <stdint.h>

constexpr unsigned TSCH = 3440;
constexpr unsigned TOFF = 101;

constexpr int B_ = 4, HQ = 32, HKV = 8, SKV = 8192, D = 128, GRP = 64, NG = SKV / GRP, REP = HQ / HKV;
constexpr int NBH = B_ * HKV, NSPLIT = 8, NWG = NBH * NSPLIT, WAVES = 4, NTHR = WAVES * 64, NST = 16;
constexpr unsigned KVBYTES = (unsigned)B_ * HKV * SKV * D * 4u, QBYTES = B_ * HQ * D * 2u;
constexpr unsigned KPBYTES = (unsigned)B_ * HKV * NG * D * 2u, VPBYTES = (unsigned)B_ * HKV * SKV * 2u;
constexpr unsigned OOB = 0x80000000u;
constexpr int L_KS = 0, L_KZ = 4096, L_VS = 8192, L_VZ = 10240, L_Y = 12288, L_M = L_Y + WAVES * 4096, L_Q = L_M + 256, L_T = L_Q + 1024;
constexpr int TROW = 272, TSZ = 16 * TROW;
constexpr int L_END = L_T + WAVES * TSZ;
constexpr unsigned WS_EP = 0, WS_T0 = 8192, WS_ML = 16384, WS_REC = WS_ML + NBH * REP * 1024, WS_PAD = WS_REC + NBH * REP * 8192,
                   WS_END = WS_PAD + NWG * 128;

typedef int i32x4 __attribute__((ext_vector_type(4)));
typedef unsigned u32x4 __attribute__((ext_vector_type(4)));
typedef unsigned u32x2 __attribute__((ext_vector_type(2)));
typedef float f32x4 __attribute__((ext_vector_type(4)));
typedef float f32x2 __attribute__((ext_vector_type(2)));
typedef float f32x16 __attribute__((ext_vector_type(16)));
typedef __bf16 bf16x8 __attribute__((ext_vector_type(8)));
typedef __bf16 bf16x2 __attribute__((ext_vector_type(2)));

__device__ __forceinline__ unsigned pk_bf16(float lo, float hi) {
  f32x2 v = {lo, hi};
  return __builtin_bit_cast(unsigned, __builtin_convertvector(v, bf16x2));
}
__device__ __forceinline__ float rnd_bf16(float x) { return __builtin_bit_cast(float, pk_bf16(0.f, x)); }
__device__ __forceinline__ float bf_lo(unsigned u) { return __builtin_bit_cast(float, u << 16); }
__device__ __forceinline__ float bf_hi(unsigned u) { return __builtin_bit_cast(float, u & 0xffff0000u); }
__device__ __forceinline__ f32x4 mfma16(u32x4 a, u32x4 b, f32x4 c) {
  return __builtin_amdgcn_mfma_f32_16x16x32_bf16(__builtin_bit_cast(bf16x8, a), __builtin_bit_cast(bf16x8, b), c, 0, 0, 0);
}
__device__ __forceinline__ f32x16 mfma32(u32x4 a, u32x4 b, f32x16 c) {
  return __builtin_amdgcn_mfma_f32_32x32x16_bf16(__builtin_bit_cast(bf16x8, a), __builtin_bit_cast(bf16x8, b), c, 0, 0, 0);
}
__device__ __forceinline__ float xg_max(float x) {
  auto s = __builtin_amdgcn_permlane16_swap(__float_as_uint(x), __float_as_uint(x), false, false);
  x = fmaxf(__uint_as_float(s[0]), __uint_as_float(s[1]));
  auto t = __builtin_amdgcn_permlane32_swap(__float_as_uint(x), __float_as_uint(x), false, false);
  return fmaxf(__uint_as_float(t[0]), __uint_as_float(t[1]));
}
__device__ __forceinline__ float xg_sum(float x) {
  auto s = __builtin_amdgcn_permlane16_swap(__float_as_uint(x), __float_as_uint(x), false, false);
  x = __uint_as_float(s[0]) + __uint_as_float(s[1]);
  auto t = __builtin_amdgcn_permlane32_swap(__float_as_uint(x), __float_as_uint(x), false, false);
  return __uint_as_float(t[0]) + __uint_as_float(t[1]);
}
__device__ __forceinline__ float ror8(float x) {
  return __builtin_bit_cast(float, __builtin_amdgcn_mov_dpp(__builtin_bit_cast(int, x), 0x128, 0xf, 0xf, true));
}
__device__ __forceinline__ __amdgpu_buffer_rsrc_t mkrsrc(const void* p, unsigned bytes) {
  return __builtin_amdgcn_make_buffer_rsrc(const_cast<void*>(p), 0, (int)bytes, 0x00020000);
}
__device__ __forceinline__ unsigned now32() {
  unsigned long long t; asm volatile("s_memrealtime %0\n\ts_waitcnt lgkmcnt(0)" : "=s"(t) :: "memory"); return (unsigned)t;
}
__device__ __forceinline__ unsigned group_of(unsigned sp, unsigned lg) { return (lg & 1) | ((sp & 1) << 1) | ((lg & 14) << 1) | ((sp & 6) << 4); }
__device__ __forceinline__ unsigned local_group(unsigned step, unsigned bl) { return (step + 14 * bl) & 15; }
__device__ __forceinline__ unsigned tile_perm(unsigned wave) { return (wave * 2 + 7 * blockIdx.x) & 7; }

#define SB() __builtin_amdgcn_sched_barrier(0)
#define THROTTLE(A, B, C)                                                                                              \
  asm volatile("s_cmp_gt_i32 %0, %5\n\ts_cbranch_scc1 1f\n\ts_waitcnt vmcnt(%3)\n\ts_cmp_gt_i32 %0, %4\n\ts_cbranch_scc1 1f\n\t" \
               "s_waitcnt vmcnt(%2)\n\ts_cmp_gt_i32 %0, 0\n\ts_cbranch_scc1 1f\n\ts_waitcnt vmcnt(%1)\n1:"             \
               :: "s"(lag), "n"((A) - 1), "n"((B) - 1), "n"((C) - 1), "n"(15 * 256), "n"(60 * 256) : "scc", "memory")

__global__ void __launch_bounds__(NTHR, 2)
kv_decode(const uint16_t* __restrict__ q, const int* __restrict__ qk, const int* __restrict__ qv,
          const uint16_t* __restrict__ ks, const uint16_t* __restrict__ kz,
          const uint16_t* __restrict__ vs, const uint16_t* __restrict__ vz,
          unsigned* ws, unsigned* out) {
  __shared__ __attribute__((aligned(16))) char lds[L_END];
  unsigned tstart = (unsigned)__builtin_amdgcn_s_memrealtime();
  asm volatile("" :: "s"(q), "s"(qk), "s"(qv), "s"(ks), "s"(kz), "s"(vs), "s"(vz), "s"(ws), "s"(out), "s"(tstart));
  const unsigned split = (blockIdx.x >> 3) & 7, bl = blockIdx.x >> 6, bh = (blockIdx.x & 7) * 4 + bl;
  const int tid = threadIdx.x, lane = tid & 63;
  const unsigned wave = __builtin_amdgcn_readfirstlane(tid >> 6);
  const int c = lane & 15, g4 = lane >> 4, hh = lane >> 5, ch = lane & 31;
  const __amdgpu_buffer_rsrc_t rws = mkrsrc(ws, WS_END);
  const unsigned row = bh * REP + wave;
  const unsigned rowc = ((bh + 16) & (NBH - 1)) * REP + wave;

  const unsigned ko = group_of(split, tid >> 4) * 256 + (tid & 15) * 16;
  const u32x4 st0 = __builtin_amdgcn_raw_buffer_load_b128(mkrsrc(ks, KPBYTES), ko, bh * (NG * D * 2), 2);
  const u32x4 st1 = __builtin_amdgcn_raw_buffer_load_b128(mkrsrc(kz, KPBYTES), ko, bh * (NG * D * 2), 2);
  const u32x4 st2 = __builtin_amdgcn_raw_buffer_load_b128(mkrsrc(wave < 2 ? vs : vz, VPBYTES),
                                                          group_of(split, (tid & 127) >> 3) * 128 + (tid & 7) * 16, bh * (SKV * 2), 2);
  const unsigned st3 = __builtin_amdgcn_raw_buffer_load_b32(mkrsrc(q, QBYTES), tid * 4, bh * 1024, 2);
  SB();

  const unsigned vo = lane * 16, perm = tile_perm(wave);
  u32x4 buf[8];
  auto tile_off = [&](unsigned lg) -> unsigned { return (((bh * NG + group_of(split, lg)) * 4 + wave) * 8 + perm) * 1024u; };
  auto issue = [&](int n, const int* base, unsigned bytes, unsigned so) __attribute__((always_inline)) {
    buf[n] = __builtin_amdgcn_raw_buffer_load_b128(mkrsrc(base, bytes), vo, so ^ (n * 1024u), 2);
  };
  unsigned lg = local_group(0, bl);
#pragma unroll
  for (int i = 0; i < 8; ++i) issue(i, qk, KVBYTES, tile_off(lg));
  SB();
  const unsigned pre = __builtin_amdgcn_raw_buffer_load_b32(
      mkrsrc(wave == 0 ? (const void*)qv : wave == 1 ? (const void*)out : (const void*)ws, wave == 0 ? KVBYTES : wave == 1 ? QBYTES : wave == 2 ? WS_END : 0u),
      lane == 0 ? 0u : OOB, wave == 0 ? tile_off(lg) : wave == 1 ? rowc * 256 + split * 32 : WS_PAD + blockIdx.x * 128, 2);
  if (blockIdx.x < 8 && wave == 3) __builtin_amdgcn_raw_buffer_store_b32(tstart, rws, lane == 0 ? 0u : OOB, WS_T0 + blockIdx.x * 4, 16);
  *reinterpret_cast<u32x4*>(lds + L_KS + tid * 16) = st0;
  *reinterpret_cast<u32x4*>(lds + L_KZ + tid * 16) = st1;
  {
    const unsigned pc = tile_perm((tid >> 1) & 3);
    const u32x4 a = pc & 1 ? u32x4{st2[1], st2[0], st2[3], st2[2]} : st2;
    const u32x4 b = pc & 2 ? u32x4{a[2], a[3], a[0], a[1]} : a;
    *reinterpret_cast<u32x4*>(lds + L_VS + (tid ^ (pc >> 2)) * 16) = b;
  }
  *reinterpret_cast<unsigned*>(lds + L_Q + tid * 4) = st3;
  __syncthreads();
  u32x4 Bq[4];
#pragma unroll
  for (int m = 0; m < 4; ++m) Bq[m] = *reinterpret_cast<const u32x4*>(lds + L_Q + (c & 3) * 256 + g4 * 16 + m * 64);

  char* const tw = lds + L_T + wave * TSZ + hh * 8 * TROW + ch * 8;
  const char* const tr = lds + L_T + wave * TSZ + c * TROW + g4 * 16;
  char* const yb = lds + L_Y + wave * 4096 + (g4 * 4 + (c & 3)) * 16;
  constexpr float SCALE = 0.088388347648318440f, LOG2E = 1.4426950408889634f;
  int lag;
  auto check = [&](unsigned idx) __attribute__((always_inline)) {
    lag = (int)((now32() - tstart - TOFF) * 256u - (idx - 5) * TSCH);
  };

  f32x16 acc[4];
#pragma unroll
  for (int cc = 0; cc < 4; ++cc) {
#pragma unroll
    for (int r = 0; r < 16; ++r) acc[cc][r] = 0.f;
    asm volatile("" : "+v"(acc[cc]));
  }

  float mx = -3.0e38f;
  unsigned tpeer = 0;
#pragma clang loop unroll(disable)
  for (unsigned it = 0; it < NST; ++it) {
    const u32x2 sraw = *reinterpret_cast<const u32x2*>(lds + L_KS + lg * 256 + ch * 8);
    const u32x2 zraw = *reinterpret_cast<const u32x2*>(lds + L_KZ + lg * 256 + ch * 8);
    const f32x2 s01 = {bf_lo(sraw[0]), bf_hi(sraw[0])}, s23 = {bf_lo(sraw[1]), bf_hi(sraw[1])};
    const f32x2 z01 = {bf_lo(zraw[0]), bf_hi(zraw[0])}, z23 = {bf_lo(zraw[1]), bf_hi(zraw[1])};
    lg = local_group(it + 1, bl);
    const unsigned so_nxt = tile_off(lg);
    const int* const nb = it + 1 < NST ? qk : qv;
    if (it == 2) {
      int d = (int)(tpeer - tstart);
      d = lane < 8 ? (d > -400 && d < 400 ? d : 0) : -1000;
      d = max(d, __builtin_amdgcn_mov_dpp(d, 0xB1, 0xf, 0xf, true));
      d = max(d, __builtin_amdgcn_mov_dpp(d, 0x4E, 0xf, 0xf, true));
      d = max(d, __builtin_amdgcn_mov_dpp(d, 0x141, 0xf, 0xf, true));
      tstart += __builtin_amdgcn_readfirstlane(d);
    }
    if (it == 1) tpeer = __builtin_amdgcn_raw_buffer_load_b32(rws, lane < 8 ? lane * 4u : OOB, WS_T0, 16);
    SB();
#pragma unroll
    for (int n = 0; n < 8; ++n) {
      const i32x4 a = __builtin_bit_cast(i32x4, buf[n]);
      f32x2 t01 = {(float)a[0], (float)a[1]}, t23 = {(float)a[2], (float)a[3]};
      asm volatile("" : "+v"(t01), "+v"(t23));
      SB();
      if ((n & 3) == 0) check(it * 8 + n + 8);
      THROTTLE(4, 6, 7);
      issue(n, nb, KVBYTES, so_nxt);
      SB();
      t01 -= z01; t23 -= z23;
      t01 = f32x2{rnd_bf16(t01[0]), rnd_bf16(t01[1])} * s01;
      t23 = f32x2{rnd_bf16(t23[0]), rnd_bf16(t23[1])} * s23;
      *reinterpret_cast<u32x2*>(tw + n * TROW) = u32x2{pk_bf16(t01[0], t01[1]), pk_bf16(t23[0], t23[1])};
    }
    asm volatile("" ::: "memory");
    f32x4 S = {0.f, 0.f, 0.f, 0.f};
#pragma unroll
    for (int m = 0; m < 4; ++m) S = mfma16(*reinterpret_cast<const u32x4*>(tr + m * 64), Bq[m], S);
    asm volatile("" ::: "memory");
    f32x4 y;
#pragma unroll
    for (int r = 0; r < 4; ++r) y[r] = rnd_bf16(rnd_bf16(S[r]) * SCALE);
    mx = fmaxf(fmaxf(mx, fmaxf(y[0], y[1])), fmaxf(y[2], y[3]));
    *reinterpret_cast<f32x4*>(yb + it * 256) = y;
  }
  const unsigned epp = __builtin_amdgcn_raw_buffer_load_b32(rws, (lane >> 3) * 4, WS_EP + row * 32, 16);
  const unsigned epc = __builtin_amdgcn_raw_buffer_load_b32(rws, 0, WS_EP + rowc * 32 + split * 4, 16);
  const unsigned touch = __builtin_amdgcn_raw_buffer_load_b32(mkrsrc(out, QBYTES), lane == 0 ? 0u : OOB, rowc * 256 + split * 32, 0);
  asm volatile("" :: "v"(pre));
  mx = xg_max(mx);
  if (lane < 4) *reinterpret_cast<float*>(lds + L_M + (lane * 4 + wave) * 4) = mx;
  __syncthreads();
  {
    const f32x4 m4 = *reinterpret_cast<const f32x4*>(lds + L_M + (c & 3) * 16);
    mx = fmaxf(fmaxf(m4[0], m4[1]), fmaxf(m4[2], m4[3]));
  }

  const bool lo_lane = (c >> 2) & 1;
  const unsigned sel = hh ? 0x03020c0cu : 0x01000c0cu;
  float l_run = 0.f, z_run = 0.f;
  float* const rec = reinterpret_cast<float*>(lds + L_T + wave * TSZ);
#pragma clang loop unroll(disable)
  for (unsigned it = 0; it < NST; ++it) {
    const f32x4 y = *reinterpret_cast<const f32x4*>(yb + it * 256);
    const u32x4 vsr = *reinterpret_cast<const u32x4*>(lds + L_VS + lg * 128 + wave * 32 + (g4 & 1) * 16);
    const u32x4 vzr = *reinterpret_cast<const u32x4*>(lds + L_VZ + lg * 128 + wave * 32 + (g4 & 1) * 16);
    lg = local_group(it + 1, bl);
    const unsigned so_nxt = tile_off(lg);
    const unsigned nbytes = it + 1 < NST ? KVBYTES : 0u;
    u32x4 Bv[4];
    auto vpair = [&](int u) __attribute__((always_inline)) {
      SB();
      const i32x4 a = __builtin_bit_cast(i32x4, buf[2 * u]);
      f32x4 fa = {(float)a[0], (float)a[1], (float)a[2], (float)a[3]};
      asm volatile("" : "+v"(fa));
      SB();
      if ((u & 1) == 0) {
        check(128 + it * 8 + 2 * u + 8);
        lag = (nbytes == 0 || (it >= 11 && lag > 30 * 256)) ? 1 << 30 : lag;
      }
      THROTTLE(4, 5, 6);
      issue(2 * u, qv, nbytes, so_nxt);
      SB();
      const i32x4 b = __builtin_bit_cast(i32x4, buf[2 * u + 1]);
#pragma unroll
      for (int cc = 0; cc < 4; ++cc) Bv[cc][u] = pk_bf16(fa[cc], (float)b[cc]);
      SB();
      THROTTLE(4, 5, 6);
      issue(2 * u + 1, qv, nbytes, so_nxt);
      SB();
    };
    vpair(0);
    float p[4];
#pragma unroll
    for (int r = 0; r < 4; ++r) p[r] = __builtin_amdgcn_exp2f((y[r] - mx) * LOG2E);
    l_run += (p[0] + p[1]) + (p[2] + p[3]);
    float pv[4];
#pragma unroll
    for (int r = 0; r < 4; ++r) {
      pv[r] = p[r] * __builtin_bit_cast(float, __builtin_amdgcn_perm(vsr[r], vsr[r], sel));
      z_run = fmaf(pv[r], __builtin_bit_cast(float, __builtin_amdgcn_perm(vzr[r], vzr[r], sel)), z_run);
    }
    vpair(1);
    unsigned X[2];
#pragma unroll
    for (int u = 0; u < 2; ++u) {
      const unsigned h = pk_bf16(pv[2 * u], pv[2 * u + 1]);
      const unsigned l = pk_bf16(pv[2 * u] - bf_lo(h), pv[2 * u + 1] - bf_hi(h));
      X[u] = lo_lane ? l : h;
    }
    const auto w0 = __builtin_amdgcn_permlane16_swap(X[0], X[0], false, false);
    const auto w1 = __builtin_amdgcn_permlane16_swap(X[1], X[1], false, false);
    const u32x4 P = {w0[0], w1[0], w0[1], w1[1]};
    vpair(2);
    *reinterpret_cast<f32x2*>(rec + 1024 + 2 * (c & 3)) = f32x2{xg_sum(l_run), xg_sum(z_run)};
    vpair(3);
#pragma unroll
    for (int cc = 0; cc < 4; ++cc) acc[cc] = mfma32(P, Bv[cc], acc[cc]);
  }

  const unsigned tagp = epp + 0x9E3779B9u, tag = __builtin_amdgcn_readfirstlane(epc) + 0x9E3779B9u;
  asm volatile("" :: "v"(touch));
  u32x4 g = __builtin_amdgcn_raw_buffer_load_b128(rws, lane * 16, WS_REC + rowc * 8192 + split * 1024, 16);
  u32x4 ml = __builtin_amdgcn_raw_buffer_load_b128(rws, (lane >> 3) * 16, WS_ML + rowc * 1024 + split * 128, 16);
  SB();
#pragma unroll
  for (int r = 0; r < 4; ++r)
    *reinterpret_cast<f32x4*>(rec + (hh * 4 + r) * D + 4 * ch) = f32x4{acc[0][r], acc[1][r], acc[2][r], acc[3][r]};
  const float M = __builtin_bit_cast(float, __builtin_amdgcn_readlane(__builtin_bit_cast(int, mx), wave));
  __syncthreads();
  f32x2 lz[WAVES], hi[WAVES], lo[WAVES];
#pragma unroll
  for (int w = 0; w < WAVES; ++w) {
    const float* rw = reinterpret_cast<const float*>(lds + L_T + w * TSZ);
    hi[w] = *reinterpret_cast<const f32x2*>(rw + wave * D + 2 * lane);
    lo[w] = *reinterpret_cast<const f32x2*>(rw + (4 + wave) * D + 2 * lane);
    lz[w] = *reinterpret_cast<const f32x2*>(rw + 1024 + 2 * wave);
  }
  SB();
  const f32x2 LZ = (lz[0] + lz[1]) + (lz[2] + lz[3]);
  const f32x2 o = (((hi[0] + lo[0]) + (hi[1] + lo[1])) + ((hi[2] + lo[2]) + (hi[3] + lo[3]))) - LZ[1];
  __builtin_amdgcn_raw_buffer_store_b128(u32x4{__float_as_uint(o[0]), tagp, __float_as_uint(o[1]), tagp}, rws,
                                         (lane >> 3) * 1024 + (lane & 7) * 16, WS_REC + row * 8192 + split * 128, 16);
  __builtin_amdgcn_raw_buffer_store_b128(u32x4{__float_as_uint(M), tagp, __float_as_uint(LZ[0]), tagp}, rws,
                                         (lane & 7) == 0 ? lane * 16u : OOB, WS_ML + row * 1024 + split * 16, 16);
  bool need = !(g[1] == tag && g[3] == tag && ml[1] == tag && ml[3] == tag);
#pragma clang loop unroll(disable)
  for (unsigned spin = 0; spin < (1u << 20); ++spin) {
    if (!__any(need)) break;
    if (need) {
      unsigned rb = rowc;
      asm volatile("" : "+s"(rb) :: "memory");
      g = __builtin_amdgcn_raw_buffer_load_b128(rws, lane * 16, WS_REC + rb * 8192 + split * 1024, 16);
      ml = __builtin_amdgcn_raw_buffer_load_b128(rws, (lane >> 3) * 16, WS_ML + rb * 1024 + split * 128, 16);
      need = !(g[1] == tag && g[3] == tag && ml[1] == tag && ml[3] == tag);
    }
  }
  const float mp = __uint_as_float(ml[0]);
  const float Mx = xg_max(fmaxf(mp, ror8(mp)));
  const float f = __builtin_amdgcn_exp2f((mp - Mx) * LOG2E);
  float a0 = f * __uint_as_float(g[0]), a1 = f * __uint_as_float(g[2]), Lx = f * __uint_as_float(ml[2]);
  a0 = xg_sum(a0 + ror8(a0));
  a1 = xg_sum(a1 + ror8(a1));
  Lx = __builtin_amdgcn_rcpf(xg_sum(Lx + ror8(Lx)));
  __builtin_amdgcn_raw_buffer_store_b32(pk_bf16(a0 * Lx, a1 * Lx), mkrsrc(out, QBYTES), lane < 8 ? lane * 4u : OOB, rowc * 256 + split * 32, 0);
  __builtin_amdgcn_raw_buffer_store_b32(tag, rws, lane == 0 ? 0u : OOB, WS_EP + rowc * 32 + split * 4, 0);
}

extern "C" void kernel_launch(void* const* d_in, const int* in_sizes, int n_in, void* d_out, int out_size,
                              void* d_ws, size_t ws_size, hipStream_t stream) {
  kv_decode<<<NWG, NTHR, 0, stream>>>((const uint16_t*)d_in[0], (const int*)d_in[1], (const int*)d_in[2],
                                      (const uint16_t*)d_in[3], (const uint16_t*)d_in[4],
                                      (const uint16_t*)d_in[5], (const uint16_t*)d_in[6],
                                      (unsigned*)d_ws, (unsigned*)d_out);
}
```
